```python
import torch

def _softmax(x, axis=-1):
    return torch.softmax(x, dim=axis)
def _gelu(x):
    return torch.nn.functional.gelu(x, approximate="tanh")
def _silu(x):
    return torch.nn.functional.silu(x)
def _sigmoid(x):
    return torch.sigmoid(x)
def _layernorm(x, axis=-1, eps=1e-5):
    m = x.mean(dim=axis, keepdim=True)
    v = x.var(dim=axis, keepdim=True, unbiased=False)
    return (x - m) / torch.sqrt(v + eps)
def _scatter_add(tgt, idx, src, axis=0):
    return tgt.clone().index_add_(axis, idx, src)
def _segment_sum(src, seg, n):
    out = torch.zeros((n,) + src.shape[1:], dtype=src.dtype)
    return out.index_add_(0, seg, src)
def _pad(x, pw):
    flat = tuple(v for pair in reversed(pw) for v in pair)
    return torch.nn.functional.pad(x, flat)

kernel_name = 'concat_heads__mha_block_cached__gqa_block_cached__near_ridge'

def setup_inputs():

    _w = (lambda a: (lambda t: t.to(torch.bfloat16) if t.is_floating_point() else t)(torch.as_tensor(a)))
    def _A_setup():
        B, Sq, D = 16, 160, 512
        H, Sk, Dh = 32, 1024, 128
        return {
            "x": _w((lambda *s: torch.randn(*s, dtype=torch.bfloat16) * 0.8)(B, Sq, D)),
            "k_cache": _w((lambda *s: torch.randn(*s, dtype=torch.bfloat16) * 0.8)(B, H, Sk, Dh)),
            "v_cache": _w((lambda *s: torch.randn(*s, dtype=torch.bfloat16) * 0.05)(B, H, Sk, Dh)),
            "wq": _w((lambda *s: torch.randn(*s, dtype=torch.bfloat16) * 0.8)(D, H * Dh) * (D ** -0.5)),
            "wo": _w((lambda *s: torch.randn(*s, dtype=torch.bfloat16) * 0.05)(H * Dh, D) * ((H * Dh) ** -0.5)),
        }
    _A_in = _A_setup()

    def _B_setup():
        B, Sq, D = 16, 160, 512
        Hq, Hkv, Sk, Dh = 32, 32, 1024, 128
        assert Hq % Hkv == 0, (Hq, Hkv)
        return {
            "x": _w((lambda *s: torch.randn(*s, dtype=torch.bfloat16) * 0.8)(B, Sq, D)),
            "k_cache": _w((lambda *s: torch.randn(*s, dtype=torch.bfloat16) * 0.8)(B, Hkv, Sk, Dh)),
            "v_cache": _w((lambda *s: torch.randn(*s, dtype=torch.bfloat16) * 0.05)(B, Hkv, Sk, Dh)),
            "wq": _w((lambda *s: torch.randn(*s, dtype=torch.bfloat16) * 0.8)(D, Hq * Dh) * (D ** -0.5)),
            "wo": _w((lambda *s: torch.randn(*s, dtype=torch.bfloat16) * 0.05)(Hq * Dh, D) * ((Hq * Dh) ** -0.5)),
        }
    _B_in = _B_setup()

    return {
        "A_x": _A_in["x"],
        "A_k_cache": _A_in["k_cache"],
        "A_v_cache": _A_in["v_cache"],
        "A_wq": _A_in["wq"],
        "A_wo": _A_in["wo"],
        "B_x": _B_in["x"],
        "B_k_cache": _B_in["k_cache"],
        "B_v_cache": _B_in["v_cache"],
        "B_wq": _B_in["wq"],
        "B_wo": _B_in["wo"],
    }

def reference(A_x, A_k_cache, A_v_cache, A_wq, A_wo, B_x, B_k_cache, B_v_cache, B_wq, B_wo):
    def _A_ref(x, k_cache, v_cache, wq, wo):
        B, Sq, D = x.shape
        H, Dh = 32, 128
        q = (lambda x, *d: x.permute(*d))((x @ wq).reshape(B, Sq, H, Dh), 0, 2, 1, 3)
        scores = torch.einsum("bhqd,bhkd->bhqk", q, k_cache) * (Dh ** -0.5)
        p = _softmax(scores, axis=-1)
        o = torch.einsum("bhqk,bhkd->bhqd", p, v_cache)
        return (lambda x, *d: x.permute(*d))(o, 0, 2, 1, 3).reshape(B, Sq, H * Dh) @ wo

    def _B_ref(x, k_cache, v_cache, wq, wo):
        B, Sq, D = x.shape
        Hq, Dh = 32, 128
        Hkv = k_cache.shape[1]
        rep = Hq // Hkv
        q = (lambda x, *d: x.permute(*d))((x @ wq).reshape(B, Sq, Hq, Dh), 0, 2, 1, 3)
        k = (lambda x, n, axis: torch.repeat_interleave(x, n, dim=axis))(k_cache, rep, axis=1)
        v = (lambda x, n, axis: torch.repeat_interleave(x, n, dim=axis))(v_cache, rep, axis=1)
        scores = torch.einsum("bhqd,bhkd->bhqk", q, k) * (Dh ** -0.5)
        p = _softmax(scores, axis=-1)
        o = torch.einsum("bhqk,bhkd->bhqd", p, v)
        return (lambda x, *d: x.permute(*d))(o, 0, 2, 1, 3).reshape(B, Sq, Hq * Dh) @ wo

    a = _A_ref(A_x, A_k_cache, A_v_cache, A_wq, A_wo)
    b = _B_ref(B_x, B_k_cache, B_v_cache, B_wq, B_wo)
    return torch.concatenate([a, b], axis=-1)
```

```cpp
#include <hip/hip_runtime.h>
#include <hip/hip_bf16.h>
#include <stdint.h>

typedef short bf16x8 __attribute__((ext_vector_type(8)));
typedef short s16x4 __attribute__((ext_vector_type(4)));
typedef float f32x16 __attribute__((ext_vector_type(16)));
typedef float f32x4 __attribute__((ext_vector_type(4)));
typedef float f32x2 __attribute__((ext_vector_type(2)));
typedef __bf16 bf16x2_t __attribute__((ext_vector_type(2)));
typedef unsigned u32x4 __attribute__((ext_vector_type(4)));
typedef unsigned u32x2 __attribute__((ext_vector_type(2)));
typedef unsigned short u16;
typedef __attribute__((address_space(3))) char* lds_ptr;
typedef __attribute__((address_space(3))) const char* lds_cptr;
typedef short v4i16_t __attribute__((ext_vector_type(4)));

#define DI __device__ __forceinline__
#define MFMA32(a, b, c) __builtin_amdgcn_mfma_f32_32x32x16_bf16((a), (b), (c), 0, 0, 0)
#define MFMA16(a, b, c) __builtin_amdgcn_mfma_f32_16x16x32_bf16((a), (b), (c), 0, 0, 0)
#define STR2(x) #x
#define STR(x) STR2(x)
#define WAIT_BAR(N) asm volatile("s_waitcnt vmcnt(" STR(N) ") lgkmcnt(0)\n\ts_barrier" ::: "memory")
#define LDS_BAR() asm volatile("s_waitcnt lgkmcnt(0)\n\ts_barrier" ::: "memory")

constexpr int SQ = 160, SK = 1024, DH = 128, NH = 32, NB = 16, DM = 512, HD = 4096;
constexpr float CSC = 0.088388347648318447f * 1.4426950408889634f;
constexpr float THR_RAW = 8.0f / CSC;

DI unsigned cvtpk(float lo, float hi) {
  f32x2 v = {lo, hi};
  bf16x2_t b = __builtin_convertvector(v, bf16x2_t);
  return __builtin_bit_cast(unsigned, b);
}
DI bf16x8 pack8(float a0, float a1, float a2, float a3, float a4, float a5, float a6, float a7) {
  u32x4 w = {cvtpk(a0, a1), cvtpk(a2, a3), cvtpk(a4, a5), cvtpk(a6, a7)};
  return __builtin_bit_cast(bf16x8, w);
}
DI s16x4 trd(lds_cptr p) {
  return __builtin_bit_cast(s16x4, __builtin_amdgcn_ds_read_tr16_b64_v4i16((__attribute__((address_space(3))) v4i16_t*)p));
}
DI bf16x8 tr2(lds_cptr p0, lds_cptr p1) {
  s16x4 a = trd(p0), b = trd(p1);
  return __builtin_shufflevector(a, b, 0, 1, 2, 3, 4, 5, 6, 7);
}
DI bf16x8 ldl(lds_cptr p) { return *(const __attribute__((address_space(3))) bf16x8*)p; }
DI void dma16(const char* sbase, unsigned voff, unsigned lds_dst) {
  asm volatile("s_mov_b32 m0, %2\n\ts_nop 0\n\tglobal_load_lds_dwordx4 %1, %0" ::"s"(sbase), "v"(voff), "s"(lds_dst) : "memory");
}
DI float swap32max(float x) {
  auto r = __builtin_amdgcn_permlane32_swap(__float_as_uint(x), __float_as_uint(x), false, false);
  return fmaxf(__uint_as_float(r[0]), __uint_as_float(r[1]));
}
DI float swap16max(float x) {
  auto r = __builtin_amdgcn_permlane16_swap(__float_as_uint(x), __float_as_uint(x), false, false);
  return fmaxf(__uint_as_float(r[0]), __uint_as_float(r[1]));
}
DI float swap32sum(float x) {
  auto r = __builtin_amdgcn_permlane32_swap(__float_as_uint(x), __float_as_uint(x), false, false);
  return __uint_as_float(r[0]) + __uint_as_float(r[1]);
}
DI float swap16sum(float x) {
  auto r = __builtin_amdgcn_permlane16_swap(__float_as_uint(x), __float_as_uint(x), false, false);
  return __uint_as_float(r[0]) + __uint_as_float(r[1]);
}

constexpr unsigned SLOT = 16384, XCH = 65536, LDS1 = 81920;

__global__ __launch_bounds__(256, 2) void attn_kernel(const u16* __restrict__ xA, const u16* __restrict__ kA, const u16* __restrict__ vA,
                                                      const u16* __restrict__ wqA, const u16* __restrict__ xB, const u16* __restrict__ kB,
                                                      const u16* __restrict__ vB, const u16* __restrict__ wqB, u16* __restrict__ o_ws) {
  extern __shared__ __attribute__((aligned(16))) char smem[];
  const int tid = threadIdx.x, lane = tid & 63;
  const int w = __builtin_amdgcn_readfirstlane(tid >> 6);
  const int g = blockIdx.x, xcd = g & 7, idx = g >> 3;
  const int h = 4 * xcd + (idx & 3), b = idx >> 2;
  const unsigned lds0 = (unsigned)(uintptr_t)smem;
  const lds_cptr L = (lds_cptr)smem;
  const lds_ptr LW = (lds_ptr)smem;

  const size_t kvoff = (size_t)(b * NH + h) * SK * DH * 2;
  const char* Kp0 = (const char*)kA + kvoff;
  const char* Vp0 = (const char*)vA + kvoff;
  const char* Kp1 = (const char*)kB + kvoff;
  const char* Vp1 = (const char*)vB + kvoff;
  const char* Wp0 = (const char*)wqA + h * 256;
  const char* Wp1 = (const char*)wqB + h * 256;

  const int r32 = lane & 31, hh = lane >> 5;
  const int r16 = lane & 15, gq = lane >> 4;
  const int q4 = (lane & 15) >> 2, p4 = lane & 3, blk = (lane >> 4) & 1;

  const unsigned rowl = 8 * (w >> 1) + ((lane >> 2) & 7);
  const unsigned chs = 8 * (w & 1) + 4 * (lane >> 5) + ((lane & 3) ^ (2 * (w >> 1) + ((lane >> 4) & 1)));
  const unsigned voffKV = rowl * 256 + chs * 16;
  const unsigned voffW = rowl * 8192 + chs * 16;

  auto issue = [&](int nn) {
    const unsigned dst = lds0 + (unsigned)(nn & 3) * SLOT + (unsigned)w * 1024;
    int un = nn >= 40 ? 1 : 0;
    int j = nn - 40 * un;
    if (nn >= 80) { un = 0; j = nn - 80; }
    if (j < 8) {
      const char* s = (un ? Wp1 : Wp0) + (size_t)j * (64 * 8192);
      dma16(s, voffW, dst);
      dma16(s + 16 * 8192, voffW, dst + 4096);
      dma16(s + 32 * 8192, voffW, dst + 8192);
      dma16(s + 48 * 8192, voffW, dst + 12288);
    } else {
      const char* s = ((j & 1) ? (un ? Vp1 : Vp0) : (un ? Kp1 : Kp0)) + (size_t)((j - 8) >> 1) * 16384;
      dma16(s, voffKV, dst);
      dma16(s + 4096, voffKV, dst + 4096);
      dma16(s + 8192, voffKV, dst + 8192);
      dma16(s + 12288, voffKV, dst + 12288);
    }
  };

  const unsigned kb0 = 2048 * (r32 >> 3) + 64 * (r32 & 7) + 16 * ((0 + hh) ^ ((r32 >> 2) & 3));
  const unsigned kb1 = 2048 * (r32 >> 3) + 64 * (r32 & 7) + 16 * ((2 + hh) ^ ((r32 >> 2) & 3));
  const unsigned vb0 = 64 * (4 * hh + q4) + 16 * ((2 * blk + (p4 >> 1)) ^ (0 + hh)) + 8 * (p4 & 1);
  const unsigned vb1 = 2048 + 64 * (4 * hh + q4) + 16 * ((2 * blk + (p4 >> 1)) ^ (2 + hh)) + 8 * (p4 & 1);
  const int eg = w & 1, kh = w >> 1;
  const int pa = (0x78 >> (2 * (r16 >> 2))) & 3;
  const int pg = (0x78 >> (2 * gq)) & 3;
  const unsigned keb = 2048 * (4 * kh + (pa >> 1)) + 64 * (4 * (pa & 1) + (r16 & 3)) + 16 * (gq ^ pa);
  const unsigned veb0 = 2048 * (4 * kh + (pg >> 1)) + 64 * (4 * (pg & 1) + q4) + 16 * ((0 + (p4 >> 1)) ^ pg) + 8 * (p4 & 1);
  const unsigned veb1 = 2048 * (4 * kh + (pg >> 1)) + 64 * (4 * (pg & 1) + q4) + 16 * ((2 + (p4 >> 1)) ^ pg) + 8 * (p4 & 1);

  issue(0);
  issue(1);
  issue(2);

#pragma unroll 1
  for (int u = 0; u < 2; ++u) {
    const int n0 = 40 * u;
    const u16* xp = (u ? xB : xA) + (size_t)b * SQ * DM;
    bf16x8 qf[8];
    bf16x8 qe[4];
    {
      int ln = lane;
      asm volatile("" : "+v"(ln));
      const int r32 = ln & 31, hh = ln >> 5, q4 = (ln & 15) >> 2, p4 = ln & 3, blk = (ln >> 4) & 1;
      const int sw = ((p4 & 1) << 1) | (p4 >> 1);
      const u16* xm_p = xp + (size_t)(32 * w + r32) * DM + 32 * hh;
      const u16* xe_p = xp + (size_t)(128 + r32) * DM + 32 * hh;
      unsigned wb[4];
#pragma unroll
      for (int s1 = 0; s1 < 2; ++s1)
#pragma unroll
        for (int t = 0; t < 2; ++t)
          wb[2 * s1 + t] = 8192 * hh + 2048 * s1 + 64 * (4 * t + q4) + 16 * ((2 * blk + (sw >> 1)) ^ (2 * s1 + t)) + 8 * (sw & 1);
      bf16x8 xm[3][4], xe[3][4];
#pragma unroll
      for (int c = 0; c < 2; ++c)
#pragma unroll
        for (int s = 0; s < 4; ++s) {
          xm[c][s] = *(const bf16x8*)(xm_p + 64 * c + 8 * s);
          xe[c][s] = *(const bf16x8*)(xe_p + 64 * c + 8 * s);
        }
      f32x16 aq[4] = {};
      f32x16 ae = {};
#pragma unroll
      for (int c = 0; c < 8; ++c) {
        if (c == 0) WAIT_BAR(24);
        else if (c == 1) WAIT_BAR(32);
        else if (c == 2) WAIT_BAR(40);
        else if (c == 7) WAIT_BAR(16);
        else WAIT_BAR(24);
        if (c < 6) {
#pragma unroll
          for (int s = 0; s < 4; ++s) {
            xm[(c + 2) % 3][s] = *(const bf16x8*)(xm_p + 64 * (c + 2) + 8 * s);
            xe[(c + 2) % 3][s] = *(const bf16x8*)(xe_p + 64 * (c + 2) + 8 * s);
          }
        }
        asm volatile("" ::: "memory");
        issue(n0 + c + 3);
        const lds_cptr sl = L + (c & 3) * SLOT;
#pragma unroll
        for (int s = 0; s < 4; ++s) {
#pragma unroll
          for (int dt = 0; dt < 4; ++dt) {
            bf16x8 a = tr2(sl + wb[2 * (s & 1)] + 4096 * (s >> 1) + 512 * dt, sl + wb[2 * (s & 1) + 1] + 4096 * (s >> 1) + 512 * dt);
            aq[dt] = MFMA32(a, xm[c % 3][s], aq[dt]);
          }
          bf16x8 a = tr2(sl + wb[2 * (s & 1)] + 512 * w + 4096 * (s >> 1), sl + wb[2 * (s & 1) + 1] + 512 * w + 4096 * (s >> 1));
          ae = MFMA32(a, xe[c % 3][s], ae);
        }
      }
#pragma unroll
      for (int dt = 0; dt < 4; ++dt) {
        qf[2 * dt] = pack8(aq[dt][0], aq[dt][1], aq[dt][2], aq[dt][3], aq[dt][4], aq[dt][5], aq[dt][6], aq[dt][7]);
        qf[2 * dt + 1] = pack8(aq[dt][8], aq[dt][9], aq[dt][10], aq[dt][11], aq[dt][12], aq[dt][13], aq[dt][14], aq[dt][15]);
      }
      bf16x8 e0 = pack8(ae[0], ae[1], ae[2], ae[3], ae[4], ae[5], ae[6], ae[7]);
      bf16x8 e1 = pack8(ae[8], ae[9], ae[10], ae[11], ae[12], ae[13], ae[14], ae[15]);
      *(__attribute__((address_space(3))) bf16x8*)(LW + XCH + 256 * r32 + 16 * ((4 * w + hh) ^ (r32 & 15))) = e0;
      *(__attribute__((address_space(3))) bf16x8*)(LW + XCH + 256 * r32 + 16 * ((4 * w + 2 + hh) ^ (r32 & 15))) = e1;
    }

    f32x16 o[4] = {};
    f32x4 oe[8] = {};
    float m = -__builtin_inff(), l = 0.f, me = -__builtin_inff(), le = 0.f;
    unsigned tog = 0;
#pragma unroll 1
    for (int t = 0; t < 16; ++t) {
      WAIT_BAR(8);
      issue(n0 + 8 + 2 * t + 3);
      if (t == 0) {
        int ln = lane;
        asm volatile("" : "+v"(ln));
        const int r16 = ln & 15, gq = ln >> 4;
#pragma unroll
        for (int s = 0; s < 4; ++s) {
          const int row = 16 * eg + r16;
          qe[s] = ldl(L + XCH + 256 * row + 16 * ((4 * s + gq) ^ (row & 15)));
        }
      }
      f32x16 s0 = {}, s1 = {};
      f32x4 se0 = {}, se1 = {};
      {
        const lds_cptr k0 = L + tog + kb0, k1 = L + tog + kb1, ke = L + tog + keb;
#pragma unroll
        for (int s = 0; s < 8; ++s) {
          bf16x8 a = ldl(((s & 1) ? k1 : k0) + 512 * (s >> 1));
          s0 = MFMA32(a, qf[s], s0);
        }
#pragma unroll
        for (int s = 0; s < 8; ++s) {
          bf16x8 a = ldl(((s & 1) ? k1 : k0) + 512 * (s >> 1) + 8192);
          s1 = MFMA32(a, qf[s], s1);
        }
#pragma unroll
        for (int s = 0; s < 4; ++s) {
          bf16x8 a = ldl(ke + 512 * s);
          se0 = MFMA16(a, qe[s], se0);
          bf16x8 a2 = ldl(ke + 512 * s + 4096);
          se1 = MFMA16(a2, qe[s], se1);
        }
      }
      float pm = fmaxf(s0[0], s1[0]);
#pragma unroll
      for (int r = 1; r < 16; ++r) pm = fmaxf(pm, fmaxf(s0[r], s1[r]));
      pm = swap32max(pm);
      float pe = fmaxf(fmaxf(fmaxf(se0[0], se0[1]), fmaxf(se0[2], se0[3])), fmaxf(fmaxf(se1[0], se1[1]), fmaxf(se1[2], se1[3])));
      pe = swap16max(pe);
      pe = swap32max(pe);
      if (__any((pm > m + THR_RAW) | (pe > me + THR_RAW))) {
        const float mn = fmaxf(m, pm), men = fmaxf(me, pe);
        const float al = __builtin_amdgcn_exp2f((m - mn) * CSC), ale = __builtin_amdgcn_exp2f((me - men) * CSC);
        m = mn;
        me = men;
        l *= al;
        le *= ale;
#pragma unroll
        for (int c = 0; c < 4; ++c)
#pragma unroll
          for (int r = 0; r < 16; ++r) o[c][r] *= al;
#pragma unroll
        for (int c = 0; c < 8; ++c)
#pragma unroll
          for (int r = 0; r < 4; ++r) oe[c][r] *= ale;
      }
      const float nm = -m * CSC, nme = -me * CSC;
      float ls = 0.f;
#pragma unroll
      for (int r = 0; r < 16; ++r) {
        s0[r] = __builtin_amdgcn_exp2f(fmaf(s0[r], CSC, nm));
        s1[r] = __builtin_amdgcn_exp2f(fmaf(s1[r], CSC, nm));
        ls += s0[r] + s1[r];
      }
      l += ls;
      float lse = 0.f;
#pragma unroll
      for (int r = 0; r < 4; ++r) {
        se0[r] = __builtin_amdgcn_exp2f(fmaf(se0[r], CSC, nme));
        se1[r] = __builtin_amdgcn_exp2f(fmaf(se1[r], CSC, nme));
        lse += se0[r] + se1[r];
      }
      le += lse;
      bf16x8 pf[4];
      pf[0] = pack8(s0[0], s0[1], s0[2], s0[3], s0[4], s0[5], s0[6], s0[7]);
      pf[1] = pack8(s0[8], s0[9], s0[10], s0[11], s0[12], s0[13], s0[14], s0[15]);
      pf[2] = pack8(s1[0], s1[1], s1[2], s1[3], s1[4], s1[5], s1[6], s1[7]);
      pf[3] = pack8(s1[8], s1[9], s1[10], s1[11], s1[12], s1[13], s1[14], s1[15]);
      bf16x8 pfe = pack8(se0[0], se0[1], se0[2], se0[3], se1[0], se1[1], se1[2], se1[3]);
      WAIT_BAR(8);
      issue(n0 + 8 + 2 * t + 4);
      {
        const lds_cptr v0 = L + tog + SLOT + vb0, v1 = L + tog + SLOT + vb1;
        const lds_cptr e0 = L + tog + SLOT + veb0, e1 = L + tog + SLOT + veb1;
#pragma unroll
        for (int s = 0; s < 4; ++s)
#pragma unroll
          for (int c = 0; c < 4; ++c) {
            bf16x8 a = tr2(v0 + 4096 * s + 512 * c, v1 + 4096 * s + 512 * c);
            o[c] = MFMA32(a, pf[s], o[c]);
          }
#pragma unroll
        for (int dt = 0; dt < 8; ++dt) {
          const lds_cptr e = (dt & 1) ? e1 : e0;
          bf16x8 a = tr2(e + 512 * (dt >> 1), e + 512 * (dt >> 1) + 4096);
          oe[dt] = MFMA16(a, pfe, oe[dt]);
        }
      }
      tog ^= 2 * SLOT;
    }

    u16* ob = o_ws + (size_t)u * (NB * SQ) * HD + (size_t)(b * SQ) * HD + h * DH;
    int ln = lane;
    asm volatile("" : "+v"(ln));
    {
      const int r32 = ln & 31, hh = ln >> 5;
      l = swap32sum(l);
      const float inv = 1.0f / l;
      u16* orow = ob + (size_t)(32 * w + r32) * HD + 16 * hh;
#pragma unroll
      for (int c = 0; c < 4; ++c) {
        bf16x8 v0 = pack8(o[c][0] * inv, o[c][1] * inv, o[c][2] * inv, o[c][3] * inv, o[c][4] * inv, o[c][5] * inv, o[c][6] * inv, o[c][7] * inv);
        bf16x8 v1 = pack8(o[c][8] * inv, o[c][9] * inv, o[c][10] * inv, o[c][11] * inv, o[c][12] * inv, o[c][13] * inv, o[c][14] * inv, o[c][15] * inv);
        *(bf16x8*)(orow + 32 * c) = v0;
        *(bf16x8*)(orow + 32 * c + 8) = v1;
      }
    }
    {
      const int lane = ln, r16 = ln & 15, gq = ln >> 4;
      le = swap16sum(le);
      le = swap32sum(le);
      if (lane < 16) {
        f32x2 ml = {me, le};
        *(__attribute__((address_space(3))) f32x2*)(LW + XCH + 4096 * (w ^ 2) + 8 * lane) = ml;
      }
      LDS_BAR();
      f32x2 pml = *(const __attribute__((address_space(3))) f32x2*)(L + XCH + 4096 * w + 8 * r16);
      const float M = fmaxf(me, pml[0]);
      const float sc = __builtin_amdgcn_exp2f((me - M) * CSC), scp = __builtin_amdgcn_exp2f((pml[0] - M) * CSC);
      const float f = sc / (le * sc + pml[1] * scp);
      asm volatile("s_waitcnt lgkmcnt(0)" ::: "memory");
      f32x4 keep[4];
#pragma unroll
      for (int i = 0; i < 4; ++i) {
        f32x4 snd;
#pragma unroll
        for (int r = 0; r < 4; ++r) {
          snd[r] = (kh ? oe[i][r] : oe[4 + i][r]) * f;
          keep[i][r] = (kh ? oe[4 + i][r] : oe[i][r]) * f;
        }
        *(__attribute__((address_space(3))) f32x4*)(LW + XCH + 4096 * w + 1024 * i + 16 * lane) = snd;
      }
      LDS_BAR();
      u16* erow = ob + (size_t)(128 + 16 * eg + r16) * HD;
#pragma unroll
      for (int i = 0; i < 4; ++i) {
        f32x4 rcv = *(const __attribute__((address_space(3))) f32x4*)(L + XCH + 4096 * (w ^ 2) + 1024 * i + 16 * lane);
        const int dt = 4 * kh + i;
        u32x2 v = {cvtpk(keep[i][0] + rcv[0], keep[i][1] + rcv[1]), cvtpk(keep[i][2] + rcv[2], keep[i][3] + rcv[3])};
        const int p = 32 * (dt >> 1) + 16 * (gq & 1) + 4 * (2 * (dt & 1) + (gq >> 1));
        *(u32x2*)(erow + p) = v;
      }
      asm volatile("s_waitcnt lgkmcnt(0)" ::: "memory");
    }
  }
  asm volatile("s_waitcnt vmcnt(0)" ::: "memory");
}

constexpr unsigned S2 = 28672, NS2 = 5, LDS2 = S2 * NS2;

__global__ __launch_bounds__(256, 1) void oproj_kernel(const u16* __restrict__ o_ws, const u16* __restrict__ woA, const u16* __restrict__ woB,
                                                       u16* __restrict__ out) {
  extern __shared__ __attribute__((aligned(16))) char smem[];
  const int tid = threadIdx.x, lane = tid & 63;
  const int w = __builtin_amdgcn_readfirstlane(tid >> 6);
  const int g = blockIdx.x, xcd = g & 7, idx = g >> 3;
  const int blkk = xcd >> 2, mt = 4 * (xcd & 3) + (idx >> 3), nt8 = idx & 7;
  const unsigned lds0 = (unsigned)(uintptr_t)smem;
  const lds_cptr L = (lds_cptr)smem;
  const lds_ptr LW = (lds_ptr)smem;
  const char* Op = (const char*)(o_ws + (size_t)blkk * (NB * SQ) * HD + (size_t)(mt * 160) * HD);
  const char* Wp = (const char*)((blkk ? woB : woA) + nt8 * 64);
  const int r32 = lane & 31, hh = lane >> 5;
  const int q4 = (lane & 15) >> 2, p4 = lane & 3, blk = (lane >> 4) & 1;
  const unsigned voffO = (unsigned)(8 * w + (lane >> 3)) * (HD * 2) + 16 * ((lane & 7) ^ (4 * (w & 1) + (lane >> 4)));
  const unsigned voffW = (unsigned)(8 * w + ((lane >> 2) & 7)) * (DM * 2) + 64 * (lane >> 5) + 16 * (lane & 3);
  auto issue = [&](int c, int slot) {
    const int cc = c < 64 ? c : 0;
    const unsigned dst = lds0 + (unsigned)slot * S2 + (unsigned)w * 1024;
    const char* so = Op + (size_t)cc * 128;
#pragma unroll
    for (int j = 0; j < 5; ++j) dma16(so + (size_t)j * 32 * (HD * 2), voffO, dst + 4096 * j);
    const char* sw = Wp + (size_t)cc * 64 * (DM * 2);
    dma16(sw, voffW, dst + 20480);
    dma16(sw + 32 * (DM * 2), voffW, dst + 20480 + 4096);
  };
  const unsigned ob = 128 * r32 + 16 * ((2 * w + hh) ^ ((r32 >> 1) & 7));
  const unsigned wb = 20480 + 1024 * (4 * (w >> 1) + 2 * hh) + 64 * (4 * (w & 1) + q4) + 32 * blk + 8 * p4;
  f32x16 acc[2][5] = {};
#pragma unroll
  for (int c = 0; c < 4; ++c) issue(c, c);
  int slot = 0, islot = 4;
#pragma unroll 1
  for (int c = 0; c < 64; ++c) {
    WAIT_BAR(21);
    issue(c + 4, islot);
    const lds_cptr sl = L + slot * S2;
    bf16x8 a0 = tr2(sl + wb, sl + wb + 1024);
    bf16x8 a1 = tr2(sl + wb + 512, sl + wb + 512 + 1024);
#pragma unroll
    for (int i = 0; i < 5; ++i) {
      bf16x8 bb = ldl(sl + ob + 4096 * i);
      acc[0][i] = MFMA32(a0, bb, acc[0][i]);
      acc[1][i] = MFMA32(a1, bb, acc[1][i]);
    }
    slot = slot == 4 ? 0 : slot + 1;
    islot = islot == 4 ? 0 : islot + 1;
  }
  WAIT_BAR(0);
  const int kn = w >> 1;
  {
#pragma unroll
    for (int i = 0; i < 5; ++i)
#pragma unroll
      for (int z = 0; z < 4; ++z) {
        f32x4 v;
#pragma unroll
        for (int r = 0; r < 4; ++r) v[r] = kn ? acc[0][i][4 * z + r] : acc[1][i][4 * z + r];
        *(__attribute__((address_space(3))) f32x4*)(LW + 20480 * w + 4096 * i + 1024 * z + 16 * lane) = v;
      }
  }
  LDS_BAR();
  f32x16 k1[5];
#pragma unroll
  for (int i = 0; i < 5; ++i)
#pragma unroll
    for (int z = 0; z < 4; ++z) {
      f32x4 v = *(const __attribute__((address_space(3))) f32x4*)(L + 20480 * (w ^ 2) + 4096 * i + 1024 * z + 16 * lane);
#pragma unroll
      for (int r = 0; r < 4; ++r) k1[i][4 * z + r] = (kn ? acc[1][i][4 * z + r] : acc[0][i][4 * z + r]) + v[r];
    }
  LDS_BAR();
  const int kz = w & 1;
#pragma unroll
  for (int i = 0; i < 5; ++i)
#pragma unroll
    for (int z = 0; z < 2; ++z) {
      f32x4 v;
#pragma unroll
      for (int r = 0; r < 4; ++r) v[r] = kz ? k1[i][4 * z + r] : k1[i][8 + 4 * z + r];
      *(__attribute__((address_space(3))) f32x4*)(LW + 10240 * w + 2048 * i + 1024 * z + 16 * lane) = v;
    }
  LDS_BAR();
#pragma unroll
  for (int i = 0; i < 5; ++i) {
    u16* orow = out + (size_t)(mt * 160 + 32 * i + r32) * 1024 + blkk * 512 + nt8 * 64 + 32 * kn + 16 * kz + 4 * hh;
#pragma unroll
    for (int z = 0; z < 2; ++z) {
      f32x4 v = *(const __attribute__((address_space(3))) f32x4*)(L + 10240 * (w ^ 1) + 2048 * i + 1024 * z + 16 * lane);
      float f[4];
#pragma unroll
      for (int r = 0; r < 4; ++r) f[r] = (kz ? k1[i][8 + 4 * z + r] : k1[i][4 * z + r]) + v[r];
      u32x2 pv = {cvtpk(f[0], f[1]), cvtpk(f[2], f[3])};
      *(u32x2*)(orow + 8 * z) = pv;
    }
  }
}

extern "C" void kernel_launch(void* const* d_in, const int* in_sizes, int n_in, void* d_out, int out_size, void* d_ws, size_t ws_size,
                              hipStream_t stream) {
  const u16* xA = (const u16*)d_in[0];
  const u16* kA = (const u16*)d_in[1];
  const u16* vA = (const u16*)d_in[2];
  const u16* wqA = (const u16*)d_in[3];
  const u16* woA = (const u16*)d_in[4];
  const u16* xB = (const u16*)d_in[5];
  const u16* kB = (const u16*)d_in[6];
  const u16* vB = (const u16*)d_in[7];
  const u16* wqB = (const u16*)d_in[8];
  const u16* woB = (const u16*)d_in[9];
  u16* o_ws = (u16*)d_ws;
  (void)hipFuncSetAttribute((const void*)attn_kernel, hipFuncAttributeMaxDynamicSharedMemorySize, (int)LDS1);
  (void)hipFuncSetAttribute((const void*)oproj_kernel, hipFuncAttributeMaxDynamicSharedMemorySize, (int)LDS2);
  hipLaunchKernelGGL(attn_kernel, dim3(512), dim3(256), LDS1, stream, xA, kA, vA, wqA, xB, kB, vB, wqB, o_ws);
  hipLaunchKernelGGL(oproj_kernel, dim3(256), dim3(256), LDS2, stream, (const u16*)o_ws, woA, woB, (u16*)d_out);
}
```

```cpp
#include <hip/hip_runtime.h>
#include <hip/hip_bf16.h>
#include <stdint.h>
#include <type_traits>

typedef short bf16x8 __attribute__((ext_vector_type(8)));
typedef short s16x4 __attribute__((ext_vector_type(4)));
typedef float f32x16 __attribute__((ext_vector_type(16)));
typedef float f32x4 __attribute__((ext_vector_type(4)));
typedef float f32x2 __attribute__((ext_vector_type(2)));
typedef __bf16 bf16x2_t __attribute__((ext_vector_type(2)));
typedef unsigned u32x4 __attribute__((ext_vector_type(4)));
typedef unsigned u32x2 __attribute__((ext_vector_type(2)));
typedef unsigned short u16;
typedef __attribute__((address_space(3))) char* lds_ptr;
typedef __attribute__((address_space(3))) const char* lds_cptr;
typedef short v4i16_t __attribute__((ext_vector_type(4)));

#define DMAPOS 0
#define DMA_AUX ""
#define STAMP(i)
#define DI __device__ __forceinline__
#define MFMA32(a, b, c) __builtin_amdgcn_mfma_f32_32x32x16_bf16((a), (b), (c), 0, 0, 0)
#define MFMA16(a, b, c) __builtin_amdgcn_mfma_f32_16x16x32_bf16((a), (b), (c), 0, 0, 0)
#define STR2(x) #x
#define STR(x) STR2(x)
#define WAIT_BAR(N) asm volatile("s_waitcnt vmcnt(" STR(N) ") lgkmcnt(0)\n\ts_barrier" ::: "memory")
#define LDS_BAR() asm volatile("s_waitcnt lgkmcnt(0)\n\ts_barrier" ::: "memory")

constexpr int SQ = 160, SK = 1024, DH = 128, NH = 32, NB = 16, DM = 512, HD = 4096;
constexpr float CSC = 0.088388347648318447f * 1.4426950408889634f;
constexpr float THR_L2 = 8.0f;

DI unsigned cvtpk(float lo, float hi) {
  f32x2 v = {lo, hi};
  bf16x2_t b = __builtin_convertvector(v, bf16x2_t);
  return __builtin_bit_cast(unsigned, b);
}
DI bf16x8 pack8(float a0, float a1, float a2, float a3, float a4, float a5, float a6, float a7) {
  u32x4 w = {cvtpk(a0, a1), cvtpk(a2, a3), cvtpk(a4, a5), cvtpk(a6, a7)};
  return __builtin_bit_cast(bf16x8, w);
}
DI s16x4 trd(lds_cptr p) {
  return __builtin_bit_cast(s16x4, __builtin_amdgcn_ds_read_tr16_b64_v4i16((__attribute__((address_space(3))) v4i16_t*)p));
}
DI bf16x8 tr2(lds_cptr p0, lds_cptr p1) {
  s16x4 a = trd(p0), b = trd(p1);
  return __builtin_shufflevector(a, b, 0, 1, 2, 3, 4, 5, 6, 7);
}
DI bf16x8 ldl(lds_cptr p) { return *(const __attribute__((address_space(3))) bf16x8*)p; }
DI void dma16(const char* sbase, unsigned voff, unsigned lds_dst) {
  asm volatile("s_mov_b32 m0, %2\n\ts_nop 0\n\tglobal_load_lds_dwordx4 %1, %0" DMA_AUX ::"s"(sbase), "v"(voff), "s"(lds_dst) : "memory");
}
DI float swap32max(float x) {
  auto r = __builtin_amdgcn_permlane32_swap(__float_as_uint(x), __float_as_uint(x), false, false);
  return fmaxf(__uint_as_float(r[0]), __uint_as_float(r[1]));
}
DI float swap16max(float x) {
  auto r = __builtin_amdgcn_permlane16_swap(__float_as_uint(x), __float_as_uint(x), false, false);
  return fmaxf(__uint_as_float(r[0]), __uint_as_float(r[1]));
}
DI float swap32sum(float x) {
  auto r = __builtin_amdgcn_permlane32_swap(__float_as_uint(x), __float_as_uint(x), false, false);
  return __uint_as_float(r[0]) + __uint_as_float(r[1]);
}
DI float swap16sum(float x) {
  auto r = __builtin_amdgcn_permlane16_swap(__float_as_uint(x), __float_as_uint(x), false, false);
  return __uint_as_float(r[0]) + __uint_as_float(r[1]);
}

constexpr unsigned SLOT = 16384, XCH = 65536, LDS1 = 81920;
#define SB() __builtin_amdgcn_sched_barrier(0)
#define MX3(a, b, c) __builtin_fmaxf(__builtin_fmaxf((a), (b)), (c))
#define PIN(x) asm volatile("" : "+v"(x))
DI int lane_id() {
  int l;
  asm volatile("v_mbcnt_lo_u32_b32 %0, -1, 0\n\tv_mbcnt_hi_u32_b32 %0, -1, %0" : "=v"(l));
  return l;
}
DI f32x16 mfma32_head(bf16x8 a, bf16x8 b, const f32x16& c) {
  f32x16 d;
  asm volatile("s_nop 1\n\tv_mfma_f32_32x32x16_bf16 %0, %1, %2, %3" : "=&v"(d) : "v"(a), "v"(b), "v"(c));
  return d;
}
DI f32x4 mfma16_head(bf16x8 a, bf16x8 b, const f32x4& c) {
  f32x4 d;
  asm volatile("s_nop 1\n\tv_mfma_f32_16x16x32_bf16 %0, %1, %2, %3" : "=&v"(d) : "v"(a), "v"(b), "v"(c));
  return d;
}

__global__ __launch_bounds__(256, 2) void attn_kernel(const u16* __restrict__ xA, const u16* __restrict__ kA, const u16* __restrict__ vA,
                                                      const u16* __restrict__ wqA, const u16* __restrict__ xB, const u16* __restrict__ kB,
                                                      const u16* __restrict__ vB, const u16* __restrict__ wqB, u16* __restrict__ o_ws) {
  extern __shared__ __attribute__((aligned(16))) char smem[];
  const int tid = threadIdx.x, lane = tid & 63;
  const int w = __builtin_amdgcn_readfirstlane(tid >> 6);
  const int g = blockIdx.x, xcd = g & 7, idx = g >> 3;
  const int h = 4 * xcd + (idx & 3), b = idx >> 2;
  const unsigned lds0 = (unsigned)(uintptr_t)smem;
  const lds_cptr L = (lds_cptr)smem;
  const lds_ptr LW = (lds_ptr)smem;

  const size_t kvoff = (size_t)(b * NH + h) * SK * DH * 2;
  const char* Kp0 = (const char*)kA + kvoff;
  const char* Vp0 = (const char*)vA + kvoff;
  const char* Kp1 = (const char*)kB + kvoff;
  const char* Vp1 = (const char*)vB + kvoff;
  const char* Wp0 = (const char*)wqA + h * 256;
  const char* Wp1 = (const char*)wqB + h * 256;

  const int r32 = lane & 31, hh = lane >> 5;
  const int r16 = lane & 15, gq = lane >> 4;
  const int q4 = (lane & 15) >> 2, p4 = lane & 3, blk = (lane >> 4) & 1;

  const unsigned rowl = 8 * (w >> 1) + ((lane >> 2) & 7);
  const unsigned chs = 8 * (w & 1) + 4 * (lane >> 5) + ((lane & 3) ^ (2 * (w >> 1) + ((lane >> 4) & 1)));
  const unsigned voffKV = rowl * 256 + chs * 16;

  auto mk_voffX = [&]() {
    const int ln = lane_id();
    return (unsigned)(8 * w + (ln >> 3)) * (DM * 2) + 16 * ((ln & 7) ^ (4 * (w & 1) + (ln >> 4)));
  };
  auto mk_voffW = [&]() {
    unsigned v = voffKV;
    asm volatile("" : "+v"(v));
    return ((v & ~255u) << 5) | (v & 255u);
  };
  const char* Xp0 = (const char*)(xA + (size_t)b * SQ * DM);
  const char* Xp1 = (const char*)(xB + (size_t)b * SQ * DM);

  const char* d_src;
  unsigned d_ps, d_dst;
  int d_kind;
  auto prep = [&](int nn) {
    d_dst = lds0 + (unsigned)(nn & 3) * SLOT + (unsigned)w * 1024;
    int un = nn >= 48 ? 1 : 0;
    int j = nn - 48 * un;
    if (nn >= 96) { un = 0; j = nn - 96; }
    if (j < 16) {
      if (j & 1) {
        d_src = (un ? Xp1 : Xp0) + 128 * (j >> 1);
        d_ps = 32u * 1024u;
        d_kind = 2;
      } else {
        d_src = (un ? Wp1 : Wp0) + (size_t)(j >> 1) * (64 * 8192);
        d_ps = 16u * 8192u;
        d_kind = 1;
      }
    } else {
      d_src = ((j & 1) ? (un ? Vp1 : Vp0) : (un ? Kp1 : Kp0)) + (size_t)((j - 16) >> 1) * 16384;
      d_ps = 4096u;
      d_kind = 0;
    }
  };
  auto piece = [&](int j) {
    const char* src = d_src + (size_t)(d_ps * (unsigned)j);
    const unsigned dst = d_dst + 4096u * (unsigned)j;
    if (d_kind == 0) dma16(src, voffKV, dst);
    else dma16(src, d_kind == 1 ? mk_voffW() : mk_voffX(), dst);
  };
  auto issue = [&](int nn) {
    prep(nn);
    piece(0);
    piece(1);
    piece(2);
    piece(3);
  };
  auto eissue = [&](int u, int c) { dma16((u ? Xp1 : Xp0) + 128 * 1024 + 128 * c, mk_voffX(), lds0 + XCH + 4096u * (unsigned)(c & 3) + (unsigned)w * 1024); };

  unsigned kb0 = lds0 + 2048 * (r32 >> 3) + 64 * (r32 & 7) + 16 * ((0 + hh) ^ ((r32 >> 2) & 3));
  unsigned kb1 = lds0 + 2048 * (r32 >> 3) + 64 * (r32 & 7) + 16 * ((2 + hh) ^ ((r32 >> 2) & 3));
  unsigned vb0 = lds0 + 64 * (4 * hh + q4) + 16 * ((2 * blk + (p4 >> 1)) ^ (0 + hh)) + 8 * (p4 & 1);
  unsigned vb1 = lds0 + 2048 + 64 * (4 * hh + q4) + 16 * ((2 * blk + (p4 >> 1)) ^ (2 + hh)) + 8 * (p4 & 1);
  const int eg = w & 1, kh = w >> 1;
  const int pa = (0x78 >> (2 * (r16 >> 2))) & 3;
  const int pg = (0x78 >> (2 * gq)) & 3;
  unsigned keb = lds0 + 2048 * (4 * kh + (pa >> 1)) + 64 * (4 * (pa & 1) + (r16 & 3)) + 16 * (gq ^ pa);
  unsigned veb0 = lds0 + 2048 * (4 * kh + (pg >> 1)) + 64 * (4 * (pg & 1) + q4) + 16 * ((0 + (p4 >> 1)) ^ pg) + 8 * (p4 & 1);
  unsigned veb1 = lds0 + 2048 * (4 * kh + (pg >> 1)) + 64 * (4 * (pg & 1) + q4) + 16 * ((2 + (p4 >> 1)) ^ pg) + 8 * (p4 & 1);

  issue(0);
  issue(1);
  issue(2);
  eissue(0, 0);
  eissue(0, 1);
  eissue(0, 2);

  auto unit = [&](auto U_) __attribute__((always_inline)) {
    constexpr int u = decltype(U_)::value;
    constexpr int n0 = 48 * u;
    STAMP(1);
    bf16x8 qf[8];
    bf16x8 qe[4];
    {
      const int ln = lane_id();
      const int r32 = ln & 31, hh = ln >> 5, q4 = (ln & 15) >> 2, p4 = ln & 3, blk = (ln >> 4) & 1;
      const int sw = ((p4 & 1) << 1) | (p4 >> 1);
      const unsigned wb0 = 2048 * hh + 64 * (0 + q4) + 16 * ((2 * blk + (sw >> 1)) ^ (2 * hh + 0)) + 8 * (sw & 1);
      const unsigned wb1 = 2048 * hh + 64 * (4 + q4) + 16 * ((2 * blk + (sw >> 1)) ^ (2 * hh + 1)) + 8 * (sw & 1);
      unsigned xb[4];
#pragma unroll
      for (int s = 0; s < 4; ++s) xb[s] = 128 * r32 + 16 * ((2 * s + hh) ^ ((r32 >> 1) & 7));
      f32x16 aq[4] = {};
      f32x16 ae = {};
#pragma unroll
      for (int c = 0; c < 8; ++c) {
        if (c == 0) WAIT_BAR(2);
        else if (c <= 5) WAIT_BAR(1);
        else WAIT_BAR(0);
        const lds_cptr sl = L + ((2 * c) & 3) * SLOT;
        const lds_cptr sle = sl + 512 * w;
        const lds_cptr sx = L + ((2 * c + 1) & 3) * SLOT + 4096 * w;
        const lds_cptr sxe = L + XCH + 4096 * (c & 3);
        if (c == 0) prep(n0 + 3);
        else prep(n0 + 2 * c + 2);
#pragma unroll
        for (int s = 0; s < 4; ++s) {
          const bf16x8 xm = ldl(sx + xb[s]);
          const bf16x8 xe = ldl(sxe + xb[s]);
#pragma unroll
          for (int dt = 0; dt < 4; ++dt) {
            bf16x8 a = tr2(sl + wb0 + 4096 * s + 512 * dt, sl + wb1 + 4096 * s + 512 * dt);
            aq[dt] = MFMA32(a, xm, aq[dt]);
            if (c == 0) {
              if (dt == 3) piece(s);
            } else {
              if (s < 2 && (dt & 1)) piece(2 * s + (dt >> 1));
              if (s == 2 && dt == 0) prep(n0 + 2 * c + 3);
              if (s >= 2 && (dt & 1)) piece(2 * (s - 2) + (dt >> 1));
            }
          }
          bf16x8 a = tr2(sle + wb0 + 4096 * s, sle + wb1 + 4096 * s);
          ae = MFMA32(a, xe, ae);
        }
        if (c + 3 < 8) eissue(u, c + 3);
      }
#pragma unroll
      for (int dt = 0; dt < 4; ++dt) {
        aq[dt] *= CSC;
        qf[2 * dt] = pack8(aq[dt][0], aq[dt][1], aq[dt][2], aq[dt][3], aq[dt][4], aq[dt][5], aq[dt][6], aq[dt][7]);
        qf[2 * dt + 1] = pack8(aq[dt][8], aq[dt][9], aq[dt][10], aq[dt][11], aq[dt][12], aq[dt][13], aq[dt][14], aq[dt][15]);
      }
      ae *= CSC;
      bf16x8 e0 = pack8(ae[0], ae[1], ae[2], ae[3], ae[4], ae[5], ae[6], ae[7]);
      bf16x8 e1 = pack8(ae[8], ae[9], ae[10], ae[11], ae[12], ae[13], ae[14], ae[15]);
      *(__attribute__((address_space(3))) bf16x8*)(LW + XCH + 256 * r32 + 16 * ((4 * w + hh) ^ (r32 & 15))) = e0;
      *(__attribute__((address_space(3))) bf16x8*)(LW + XCH + 256 * r32 + 16 * ((4 * w + 2 + hh) ^ (r32 & 15))) = e1;
    }

    STAMP(6);
    f32x16 o[4] = {};
    f32x4 oe[8] = {};
    float m = 0.f, l = 0.f, me = 0.f, le = 0.f;
    f32x16 negm = {};
    f32x4 negme = {};
#pragma unroll 1
    for (int t = 0; t < 16; ++t) {
      STAMP(7);
      if (t == 0) WAIT_BAR(4);
      else WAIT_BAR(8);
      STAMP(0);
      if (t == 0) issue(n0 + 18);
      prep(n0 + 16 + 2 * t + 3);
      if (t == 0) {
        const int ln = lane_id();
        const int r16 = ln & 15, gq = ln >> 4;
#pragma unroll
        for (int s = 0; s < 4; ++s) {
          const int row = 16 * eg + r16;
          qe[s] = ldl(L + XCH + 256 * row + 16 * ((4 * s + gq) ^ (row & 15)));
        }
      }
      const float thr = t == 0 ? -__builtin_inff() : THR_L2;
      const float lo = t == 0 ? -__builtin_inff() : 0.f;
      f32x16 s0, s1;
      f32x4 se0, se1;
      float ma, mb, la = 0.f, lb = 0.f, lf = 0.f;
      u32x4 pw[4];
      u32x4 pwe;
#define EX(x) __builtin_amdgcn_exp2f(x)
#define P2(S, i, DST) do { const float a_ = EX(S[i]), b_ = EX(S[(i) + 1]); la += a_; lb += b_; unsigned d_ = cvtpk(a_, b_); PIN(d_); PIN(la); PIN(lb); DST = d_; } while (0)
#define P2E(S, i, DST) do { const float a_ = EX(S[i]), b_ = EX(S[(i) + 1]); le += a_; lf += b_; unsigned d_ = cvtpk(a_, b_); PIN(d_); PIN(le); PIN(lf); DST = d_; } while (0)
      const lds_cptr k0 = (lds_cptr)(uintptr_t)kb0, k1 = (lds_cptr)(uintptr_t)kb1, ke = (lds_cptr)(uintptr_t)keb;
      bf16x8 kf[16], ef[8];
#define KRD(i) kf[i] = ldl((((i) & 1) ? k1 : k0) + 512 * (((i) & 7) >> 1) + 8192 * ((i) >> 3))
#define ERD(i) ef[i] = ldl(ke + 512 * ((i) >> 1) + 4096 * ((i) & 1))
      KRD(0); KRD(1); KRD(2); KRD(3);
      SB();
#pragma unroll
      for (int i = 0; i < 8; ++i) {
        if (i + 4 < 8) { KRD(i + 4); } else { ERD(i - 4); }
        if (i == 0) s0 = mfma32_head(kf[i], qf[i], negm);
        else s0 = MFMA32(kf[i], qf[i], s0);
        if (i == 2) piece(0);
        if (i == 6) piece(1);
        SB();
      }
#pragma unroll
      for (int i = 0; i < 8; ++i) {
        if (i + 4 < 8) { ERD(i + 4); } else { KRD(i + 4); }
        if (i == 0) se0 = mfma16_head(ef[i], qe[0], negme);
        else if (i == 1) se1 = mfma16_head(ef[i], qe[0], negme);
        else if (i & 1) se1 = MFMA16(ef[i], qe[i >> 1], se1);
        else se0 = MFMA16(ef[i], qe[i >> 1], se0);
        if (i == 3) { ma = MX3(s0[0], s0[1], s0[2]); mb = MX3(s0[3], s0[4], s0[5]); PIN(ma); PIN(mb); }
        if (i == 4) { ma = MX3(ma, s0[6], s0[7]); mb = MX3(mb, s0[8], s0[9]); PIN(ma); PIN(mb); }
        if (i == 5) { ma = MX3(ma, s0[10], s0[11]); mb = MX3(mb, s0[12], s0[13]); PIN(ma); PIN(mb); }
        if (i == 6) { ma = MX3(ma, s0[14], s0[15]); ma = swap32max(__builtin_fmaxf(ma, mb)); PIN(ma); }
        SB();
      }
      {
        const float pm = ma;
        if (__builtin_expect(__any(pm > thr), 0)) {
          const float d = fmaxf(pm, lo);
          const float al = __builtin_amdgcn_exp2f(-fmaxf(d, 0.f));
          m += d;
          l *= al;
#pragma unroll
          for (int c = 0; c < 4; ++c) o[c] *= al;
          s0 -= d;
#pragma unroll
          for (int r = 0; r < 16; ++r) negm[r] = -m;
        }
      }
      SB();
      float pe;
#pragma unroll
      for (int i = 0; i < 8; ++i) {
        if (i + 4 < 8) { KRD(i + 12); }
        if (i == 0) s1 = mfma32_head(kf[8 + i], qf[i], negm);
        else s1 = MFMA32(kf[8 + i], qf[i], s1);
        P2(s0, 2 * i, pw[i >> 2][i & 3]);
        if (i == 1) piece(2);
        if (i == 5) piece(3);
        if (i == 2) { pe = MX3(se0[0], se0[1], se0[2]); pe = MX3(pe, se0[3], se1[0]); PIN(pe); }
        if (i == 3) { pe = MX3(pe, se1[1], se1[2]); pe = __builtin_fmaxf(pe, se1[3]); PIN(pe); }
        if (i == 4) { pe = swap16max(pe); PIN(pe); }
        if (i == 6) { pe = swap32max(pe); PIN(pe); }
        SB();
      }
#undef KRD
#undef ERD
      {
        ma = MX3(s1[0], s1[1], s1[2]); mb = MX3(s1[3], s1[4], s1[5]);
        ma = MX3(ma, s1[6], s1[7]); mb = MX3(mb, s1[8], s1[9]);
        ma = MX3(ma, s1[10], s1[11]); mb = MX3(mb, s1[12], s1[13]);
        ma = MX3(ma, s1[14], s1[15]);
        const float pm = swap32max(__builtin_fmaxf(ma, mb));
        if (__builtin_expect(__any((pm > THR_L2) | (pe > thr)), 0)) {
          const float d = fmaxf(pm, 0.f);
          const float al = __builtin_amdgcn_exp2f(-d);
          m += d;
          l *= al;
          la *= al;
          lb *= al;
#pragma unroll
          for (int c = 0; c < 4; ++c) o[c] *= al;
#pragma unroll
          for (int i = 0; i < 8; ++i) {
            const unsigned v = pw[i >> 2][i & 3];
            pw[i >> 2][i & 3] = cvtpk(__uint_as_float(v << 16) * al, __uint_as_float(v & 0xffff0000u) * al);
          }
          s1 -= d;
#pragma unroll
          for (int r = 0; r < 16; ++r) negm[r] = -m;
          const float de = fmaxf(pe, lo);
          const float ale = __builtin_amdgcn_exp2f(-fmaxf(de, 0.f));
          me += de;
          le *= ale;
#pragma unroll
          for (int c = 0; c < 8; ++c) oe[c] *= ale;
          se0 -= de;
          se1 -= de;
#pragma unroll
          for (int r = 0; r < 4; ++r) negme[r] = -me;
        }
      }
      STAMP(2);
      WAIT_BAR(8);
      STAMP(3);
      prep(n0 + 16 + 2 * t + 4);
      {
        const lds_cptr v0 = (lds_cptr)(uintptr_t)vb0 + SLOT, v1 = (lds_cptr)(uintptr_t)vb1 + SLOT;
        const lds_cptr e0 = (lds_cptr)(uintptr_t)veb0 + SLOT, e1 = (lds_cptr)(uintptr_t)veb1 + SLOT;
        bf16x8 vf[16], wf[8];
#define VRD(i) vf[i] = tr2(v0 + 4096 * ((i) >> 2) + 512 * ((i) & 3), v1 + 4096 * ((i) >> 2) + 512 * ((i) & 3))
#define WRD(i) wf[i] = tr2((((i) & 1) ? e1 : e0) + 512 * ((i) >> 1), (((i) & 1) ? e1 : e0) + 512 * ((i) >> 1) + 4096)
        VRD(0); VRD(1); VRD(2);
        SB();
#pragma unroll
        for (int i = 0; i < 16; ++i) {
          const int s = i >> 2, c = i & 3;
          if (i + 3 < 16) { VRD(i + 3); } else { WRD(i - 13); }
          o[c] = MFMA32(vf[i], __builtin_bit_cast(bf16x8, pw[s]), o[c]);
          if (i < 8) P2(s1, 2 * i, pw[2 + (i >> 2)][i & 3]);
          if (i >= 8 && (i & 1) == 0) { const int j = (i - 8) >> 1; if (j < 2) P2E(se0, 2 * j, pwe[j]); else P2E(se1, 2 * j - 4, pwe[j]); }
          if (c == 1) piece(s);
          SB();
        }
#pragma unroll
        for (int i = 0; i < 8; ++i) {
          if (i + 3 < 8) { WRD(i + 3); }
          oe[i] = MFMA16(wf[i], __builtin_bit_cast(bf16x8, pwe), oe[i]);
          SB();
        }
#undef VRD
#undef WRD
      }
      l += la + lb;
      le += lf;
#undef EX
#undef P2
#undef P2E
      STAMP(5);
      const unsigned dir = (t & 1) ? 0u - 2 * SLOT : 2 * SLOT;
      kb0 += dir; kb1 += dir; keb += dir; vb0 += dir; vb1 += dir; veb0 += dir; veb1 += dir;
      PIN(kb0); PIN(kb1); PIN(keb); PIN(vb0); PIN(vb1); PIN(veb0); PIN(veb1);
    }

    u16* ob = o_ws + (size_t)u * (NB * SQ) * HD + (size_t)(b * SQ) * HD + h * DH;
    const int ln = lane_id();
    {
      const int r32 = ln & 31, hh = ln >> 5;
      l = swap32sum(l);
      const float inv = 1.0f / l;
      u16* orow = ob + (size_t)(32 * w + r32) * HD + 16 * hh;
#pragma unroll
      for (int c = 0; c < 4; ++c) {
        bf16x8 v0 = pack8(o[c][0] * inv, o[c][1] * inv, o[c][2] * inv, o[c][3] * inv, o[c][4] * inv, o[c][5] * inv, o[c][6] * inv, o[c][7] * inv);
        bf16x8 v1 = pack8(o[c][8] * inv, o[c][9] * inv, o[c][10] * inv, o[c][11] * inv, o[c][12] * inv, o[c][13] * inv, o[c][14] * inv, o[c][15] * inv);
        *(bf16x8*)(orow + 32 * c) = v0;
        *(bf16x8*)(orow + 32 * c + 8) = v1;
      }
    }
    {
      const int lane = ln, r16 = ln & 15, gq = ln >> 4;
      le = swap16sum(le);
      le = swap32sum(le);
      if (lane < 16) {
        f32x2 ml = {me, le};
        *(__attribute__((address_space(3))) f32x2*)(LW + XCH + 4096 * (w ^ 2) + 8 * lane) = ml;
      }
      LDS_BAR();
      f32x2 pml = *(const __attribute__((address_space(3))) f32x2*)(L + XCH + 4096 * w + 8 * r16);
      const float M = fmaxf(me, pml[0]);
      const float sc = __builtin_amdgcn_exp2f(me - M), scp = __builtin_amdgcn_exp2f(pml[0] - M);
      const float f = sc / (le * sc + pml[1] * scp);
      asm volatile("s_waitcnt lgkmcnt(0)" ::: "memory");
      f32x4 keep[4];
#pragma unroll
      for (int i = 0; i < 4; ++i) {
        f32x4 snd;
#pragma unroll
        for (int r = 0; r < 4; ++r) {
          snd[r] = (kh ? oe[i][r] : oe[4 + i][r]) * f;
          keep[i][r] = (kh ? oe[4 + i][r] : oe[i][r]) * f;
        }
        *(__attribute__((address_space(3))) f32x4*)(LW + XCH + 4096 * w + 1024 * i + 16 * lane) = snd;
      }
      LDS_BAR();
      u16* erow = ob + (size_t)(128 + 16 * eg + r16) * HD;
#pragma unroll
      for (int i = 0; i < 4; ++i) {
        f32x4 rcv = *(const __attribute__((address_space(3))) f32x4*)(L + XCH + 4096 * (w ^ 2) + 1024 * i + 16 * lane);
        const int dt = 4 * kh + i;
        u32x2 v = {cvtpk(keep[i][0] + rcv[0], keep[i][1] + rcv[1]), cvtpk(keep[i][2] + rcv[2], keep[i][3] + rcv[3])};
        const int p = 32 * (dt >> 1) + 16 * (gq & 1) + 4 * (2 * (dt & 1) + (gq >> 1));
        *(u32x2*)(erow + p) = v;
      }
      if (u == 0) {
        LDS_BAR();
        eissue(1, 0);
        eissue(1, 1);
        eissue(1, 2);
      }
    }
  };
  unit(std::integral_constant<int, 0>{});
  unit(std::integral_constant<int, 1>{});
  asm volatile("s_waitcnt vmcnt(0)" ::: "memory");
}

constexpr unsigned S2 = 28672, NS2 = 5, LDS2 = S2 * NS2;

__global__ __launch_bounds__(256, 1) void oproj_kernel(const u16* __restrict__ o_ws, const u16* __restrict__ woA, const u16* __restrict__ woB,
                                                       u16* __restrict__ out) {
  extern __shared__ __attribute__((aligned(16))) char smem[];
  const int tid = threadIdx.x, lane = tid & 63;
  const int w = __builtin_amdgcn_readfirstlane(tid >> 6);
  const int g = blockIdx.x, xcd = g & 7, idx = g >> 3;
  const int blkk = xcd >> 2, mt = 4 * (xcd & 3) + (idx >> 3), nt8 = idx & 7;
  const unsigned lds0 = (unsigned)(uintptr_t)smem;
  const lds_cptr L = (lds_cptr)smem;
  const lds_ptr LW = (lds_ptr)smem;
  const char* Op = (const char*)(o_ws + (size_t)blkk * (NB * SQ) * HD + (size_t)(mt * 160) * HD);
  const char* Wp = (const char*)((blkk ? woB : woA) + nt8 * 64);
  const int r32 = lane & 31, hh = lane >> 5;
  const int q4 = (lane & 15) >> 2, p4 = lane & 3, blk = (lane >> 4) & 1;
  const unsigned voffO = (unsigned)(8 * w + (lane >> 3)) * (HD * 2) + 16 * ((lane & 7) ^ (4 * (w & 1) + (lane >> 4)));
  const unsigned voffW = (unsigned)(8 * w + ((lane >> 2) & 7)) * (DM * 2) + 64 * (lane >> 5) + 16 * (lane & 3);
  auto issue = [&](int c, int slot) {
    const int cc = c < 64 ? c : 0;
    const unsigned dst = lds0 + (unsigned)slot * S2 + (unsigned)w * 1024;
    const char* so = Op + (size_t)cc * 128;
#pragma unroll
    for (int j = 0; j < 5; ++j) dma16(so + (size_t)j * 32 * (HD * 2), voffO, dst + 4096 * j);
    const char* sw = Wp + (size_t)cc * 64 * (DM * 2);
    dma16(sw, voffW, dst + 20480);
    dma16(sw + 32 * (DM * 2), voffW, dst + 20480 + 4096);
  };
  const unsigned ob = 128 * r32 + 16 * ((2 * w + hh) ^ ((r32 >> 1) & 7));
  const unsigned wb = 20480 + 1024 * (4 * (w >> 1) + 2 * hh) + 64 * (4 * (w & 1) + q4) + 32 * blk + 8 * p4;
  f32x16 acc[2][5] = {};
#pragma unroll
  for (int c = 0; c < 4; ++c) issue(c, c);
  int slot = 0, islot = 4;
#pragma unroll 1
  for (int c = 0; c < 64; ++c) {
    WAIT_BAR(21);
    issue(c + 4, islot);
    const lds_cptr sl = L + slot * S2;
    bf16x8 a0 = tr2(sl + wb, sl + wb + 1024);
    bf16x8 a1 = tr2(sl + wb + 512, sl + wb + 512 + 1024);
#pragma unroll
    for (int i = 0; i < 5; ++i) {
      bf16x8 bb = ldl(sl + ob + 4096 * i);
      acc[0][i] = MFMA32(a0, bb, acc[0][i]);
      acc[1][i] = MFMA32(a1, bb, acc[1][i]);
    }
    slot = slot == 4 ? 0 : slot + 1;
    islot = islot == 4 ? 0 : islot + 1;
  }
  WAIT_BAR(0);
  const int kn = w >> 1;
  {
#pragma unroll
    for (int i = 0; i < 5; ++i)
#pragma unroll
      for (int z = 0; z < 4; ++z) {
        f32x4 v;
#pragma unroll
        for (int r = 0; r < 4; ++r) v[r] = kn ? acc[0][i][4 * z + r] : acc[1][i][4 * z + r];
        *(__attribute__((address_space(3))) f32x4*)(LW + 20480 * w + 4096 * i + 1024 * z + 16 * lane) = v;
      }
  }
  LDS_BAR();
  f32x16 k1[5];
#pragma unroll
  for (int i = 0; i < 5; ++i)
#pragma unroll
    for (int z = 0; z < 4; ++z) {
      f32x4 v = *(const __attribute__((address_space(3))) f32x4*)(L + 20480 * (w ^ 2) + 4096 * i + 1024 * z + 16 * lane);
#pragma unroll
      for (int r = 0; r < 4; ++r) k1[i][4 * z + r] = (kn ? acc[1][i][4 * z + r] : acc[0][i][4 * z + r]) + v[r];
    }
  LDS_BAR();
  const int kz = w & 1;
#pragma unroll
  for (int i = 0; i < 5; ++i)
#pragma unroll
    for (int z = 0; z < 2; ++z) {
      f32x4 v;
#pragma unroll
      for (int r = 0; r < 4; ++r) v[r] = kz ? k1[i][4 * z + r] : k1[i][8 + 4 * z + r];
      *(__attribute__((address_space(3))) f32x4*)(LW + 10240 * w + 2048 * i + 1024 * z + 16 * lane) = v;
    }
  LDS_BAR();
#pragma unroll
  for (int i = 0; i < 5; ++i) {
    u16* orow = out + (size_t)(mt * 160 + 32 * i + r32) * 1024 + blkk * 512 + nt8 * 64 + 32 * kn + 16 * kz + 4 * hh;
#pragma unroll
    for (int z = 0; z < 2; ++z) {
      f32x4 v = *(const __attribute__((address_space(3))) f32x4*)(L + 10240 * (w ^ 1) + 2048 * i + 1024 * z + 16 * lane);
      float f[4];
#pragma unroll
      for (int r = 0; r < 4; ++r) f[r] = (kz ? k1[i][8 + 4 * z + r] : k1[i][4 * z + r]) + v[r];
      u32x2 pv = {cvtpk(f[0], f[1]), cvtpk(f[2], f[3])};
      *(u32x2*)(orow + 8 * z) = pv;
    }
  }
}

extern "C" void kernel_launch(void* const* d_in, const int* in_sizes, int n_in, void* d_out, int out_size, void* d_ws, size_t ws_size,
                              hipStream_t stream) {
  const u16* xA = (const u16*)d_in[0];
  const u16* kA = (const u16*)d_in[1];
  const u16* vA = (const u16*)d_in[2];
  const u16* wqA = (const u16*)d_in[3];
  const u16* woA = (const u16*)d_in[4];
  const u16* xB = (const u16*)d_in[5];
  const u16* kB = (const u16*)d_in[6];
  const u16* vB = (const u16*)d_in[7];
  const u16* wqB = (const u16*)d_in[8];
  const u16* woB = (const u16*)d_in[9];
  u16* o_ws = (u16*)d_ws;
  (void)hipFuncSetAttribute((const void*)attn_kernel, hipFuncAttributeMaxDynamicSharedMemorySize, (int)LDS1);
  (void)hipFuncSetAttribute((const void*)oproj_kernel, hipFuncAttributeMaxDynamicSharedMemorySize, (int)LDS2);
  hipLaunchKernelGGL(attn_kernel, dim3(512), dim3(256), LDS1, stream, xA, kA, vA, wqA, xB, kB, vB, wqB, o_ws);
  hipLaunchKernelGGL(oproj_kernel, dim3(256), dim3(256), LDS2, stream, (const u16*)o_ws, woA, woB, (u16*)d_out);
}
```

```cpp
#include <hip/hip_runtime.h>
#include <hip/hip_bf16.h>
#include <stdint.h>
#include <type_traits>

typedef short bf16x8 __attribute__((ext_vector_type(8)));
typedef short s16x4 __attribute__((ext_vector_type(4)));
typedef float f32x16 __attribute__((ext_vector_type(16)));
typedef float f32x4 __attribute__((ext_vector_type(4)));
typedef float f32x2 __attribute__((ext_vector_type(2)));
typedef __bf16 bf16x2_t __attribute__((ext_vector_type(2)));
typedef unsigned u32x4 __attribute__((ext_vector_type(4)));
typedef unsigned u32x2 __attribute__((ext_vector_type(2)));
typedef unsigned short u16;
typedef __attribute__((address_space(3))) char* lds_ptr;
typedef __attribute__((address_space(3))) const char* lds_cptr;
typedef short v4i16_t __attribute__((ext_vector_type(4)));

#define DMAPOS 0
#define KV_AUX " sc0 sc1 nt"
#define DMA_AUX ""
#define STAMP(i)
#define DI __device__ __forceinline__
#define MFMA32(a, b, c) __builtin_amdgcn_mfma_f32_32x32x16_bf16((a), (b), (c), 0, 0, 0)
#define MFMA16(a, b, c) __builtin_amdgcn_mfma_f32_16x16x32_bf16((a), (b), (c), 0, 0, 0)
#define STR2(x) #x
#define STR(x) STR2(x)
#define WAIT_BAR(N) asm volatile("s_waitcnt vmcnt(" STR(N) ") lgkmcnt(0)\n\ts_barrier" ::: "memory")
#define LDS_BAR() asm volatile("s_waitcnt lgkmcnt(0)\n\ts_barrier" ::: "memory")

constexpr int SQ = 160, SK = 1024, DH = 128, NH = 32, NB = 16, DM = 512, HD = 4096;
constexpr float CSC = 0.088388347648318447f * 1.4426950408889634f;
constexpr float THR_L2 = 8.0f;

DI unsigned cvtpk(float lo, float hi) {
  f32x2 v = {lo, hi};
  bf16x2_t b = __builtin_convertvector(v, bf16x2_t);
  return __builtin_bit_cast(unsigned, b);
}
DI bf16x8 pack8(float a0, float a1, float a2, float a3, float a4, float a5, float a6, float a7) {
  u32x4 w = {cvtpk(a0, a1), cvtpk(a2, a3), cvtpk(a4, a5), cvtpk(a6, a7)};
  return __builtin_bit_cast(bf16x8, w);
}
DI s16x4 trd(lds_cptr p) {
  return __builtin_bit_cast(s16x4, __builtin_amdgcn_ds_read_tr16_b64_v4i16((__attribute__((address_space(3))) v4i16_t*)p));
}
DI bf16x8 tr2(lds_cptr p0, lds_cptr p1) {
  s16x4 a = trd(p0), b = trd(p1);
  return __builtin_shufflevector(a, b, 0, 1, 2, 3, 4, 5, 6, 7);
}
DI bf16x8 ldl(lds_cptr p) { return *(const __attribute__((address_space(3))) bf16x8*)p; }
DI void dma16(const char* sbase, unsigned voff, unsigned lds_dst) {
  asm volatile("s_mov_b32 m0, %2\n\ts_nop 0\n\tglobal_load_lds_dwordx4 %1, %0" DMA_AUX ::"s"(sbase), "v"(voff), "s"(lds_dst) : "memory");
}
DI void dma16s(const char* sbase, unsigned voff, unsigned lds_dst) {
  asm volatile("s_mov_b32 m0, %2\n\ts_nop 0\n\tglobal_load_lds_dwordx4 %1, %0" KV_AUX ::"s"(sbase), "v"(voff), "s"(lds_dst) : "memory");
}
DI float swap32max(float x) {
  auto r = __builtin_amdgcn_permlane32_swap(__float_as_uint(x), __float_as_uint(x), false, false);
  return fmaxf(__uint_as_float(r[0]), __uint_as_float(r[1]));
}
DI float swap16max(float x) {
  auto r = __builtin_amdgcn_permlane16_swap(__float_as_uint(x), __float_as_uint(x), false, false);
  return fmaxf(__uint_as_float(r[0]), __uint_as_float(r[1]));
}
DI float swap32sum(float x) {
  auto r = __builtin_amdgcn_permlane32_swap(__float_as_uint(x), __float_as_uint(x), false, false);
  return __uint_as_float(r[0]) + __uint_as_float(r[1]);
}
DI float swap16sum(float x) {
  auto r = __builtin_amdgcn_permlane16_swap(__float_as_uint(x), __float_as_uint(x), false, false);
  return __uint_as_float(r[0]) + __uint_as_float(r[1]);
}

constexpr unsigned SLOT = 16384, XCH = 65536, LDS1 = 81920;
#define SB() __builtin_amdgcn_sched_barrier(0)
#define MX3(a, b, c) __builtin_fmaxf(__builtin_fmaxf((a), (b)), (c))
#define PIN(x) asm volatile("" : "+v"(x))
DI int lane_id() {
  int l;
  asm volatile("v_mbcnt_lo_u32_b32 %0, -1, 0\n\tv_mbcnt_hi_u32_b32 %0, -1, %0" : "=v"(l));
  return l;
}
DI f32x16 mfma32_head(bf16x8 a, bf16x8 b, const f32x16& c) {
  f32x16 d;
  asm volatile("s_nop 1\n\tv_mfma_f32_32x32x16_bf16 %0, %1, %2, %3" : "=&v"(d) : "v"(a), "v"(b), "v"(c));
  return d;
}
DI f32x4 mfma16_head(bf16x8 a, bf16x8 b, const f32x4& c) {
  f32x4 d;
  asm volatile("s_nop 1\n\tv_mfma_f32_16x16x32_bf16 %0, %1, %2, %3" : "=&v"(d) : "v"(a), "v"(b), "v"(c));
  return d;
}

__global__ __launch_bounds__(256, 2) void attn_kernel(const u16* __restrict__ xA, const u16* __restrict__ kA, const u16* __restrict__ vA,
                                                      const u16* __restrict__ wqA, const u16* __restrict__ xB, const u16* __restrict__ kB,
                                                      const u16* __restrict__ vB, const u16* __restrict__ wqB, u16* __restrict__ o_ws) {
  extern __shared__ __attribute__((aligned(16))) char smem[];
  const int tid = threadIdx.x, lane = tid & 63;
  const int w = __builtin_amdgcn_readfirstlane(tid >> 6);
  const int g = blockIdx.x, xcd = g & 7, idx = g >> 3;
  const int h = 4 * xcd + (idx & 3), b = idx >> 2;
  const unsigned lds0 = (unsigned)(uintptr_t)smem;
  const lds_cptr L = (lds_cptr)smem;
  const lds_ptr LW = (lds_ptr)smem;

  const size_t kvoff = (size_t)(b * NH + h) * SK * DH * 2;
  const char* Kp0 = (const char*)kA + kvoff;
  const char* Vp0 = (const char*)vA + kvoff;
  const char* Kp1 = (const char*)kB + kvoff;
  const char* Vp1 = (const char*)vB + kvoff;
  const char* Wp0 = (const char*)wqA + h * 256;
  const char* Wp1 = (const char*)wqB + h * 256;

  const int r32 = lane & 31, hh = lane >> 5;
  const int r16 = lane & 15, gq = lane >> 4;
  const int q4 = (lane & 15) >> 2, p4 = lane & 3, blk = (lane >> 4) & 1;

  const unsigned rowl = 8 * (w >> 1) + ((lane >> 2) & 7);
  const unsigned chs = 8 * (w & 1) + 4 * (lane >> 5) + ((lane & 3) ^ (2 * (w >> 1) + ((lane >> 4) & 1)));
  const unsigned voffKV = rowl * 256 + chs * 16;

  auto mk_voffX = [&]() {
    const int ln = lane_id();
    return (unsigned)(8 * w + (ln >> 3)) * (DM * 2) + 16 * ((ln & 7) ^ (4 * (w & 1) + (ln >> 4)));
  };
  auto mk_voffW = [&]() {
    unsigned v = voffKV;
    asm volatile("" : "+v"(v));
    return ((v & ~255u) << 5) | (v & 255u);
  };
  const char* Xp0 = (const char*)(xA + (size_t)b * SQ * DM);
  const char* Xp1 = (const char*)(xB + (size_t)b * SQ * DM);

  const char* d_src;
  unsigned d_ps, d_dst;
  int d_kind;
  auto prep = [&](int nn) {
    d_dst = lds0 + (unsigned)(nn & 3) * SLOT + (unsigned)w * 1024;
    int un = nn >= 48 ? 1 : 0;
    int j = nn - 48 * un;
    if (nn >= 96) { un = 0; j = nn - 96; }
    if (j < 16) {
      if (j & 1) {
        d_src = (un ? Xp1 : Xp0) + 128 * (j >> 1);
        d_ps = 32u * 1024u;
        d_kind = 2;
      } else {
        d_src = (un ? Wp1 : Wp0) + (size_t)(j >> 1) * (64 * 8192);
        d_ps = 16u * 8192u;
        d_kind = 1;
      }
    } else {
      d_src = ((j & 1) ? (un ? Vp1 : Vp0) : (un ? Kp1 : Kp0)) + (size_t)((j - 16) >> 1) * 16384;
      d_ps = 4096u;
      d_kind = 0;
    }
  };
  auto piece = [&](int j) {
    const char* src = d_src + (size_t)(d_ps * (unsigned)j);
    const unsigned dst = d_dst + 4096u * (unsigned)j;
    if (d_kind == 0) dma16s(src, voffKV, dst);
    else dma16(src, d_kind == 1 ? mk_voffW() : mk_voffX(), dst);
  };
  auto issue = [&](int nn) {
    prep(nn);
    piece(0);
    piece(1);
    piece(2);
    piece(3);
  };
  auto eissue = [&](int u, int c) { dma16((u ? Xp1 : Xp0) + 128 * 1024 + 128 * c, mk_voffX(), lds0 + XCH + 4096u * (unsigned)(c & 3) + (unsigned)w * 1024); };

  unsigned kb0 = lds0 + 2048 * (r32 >> 3) + 64 * (r32 & 7) + 16 * ((0 + hh) ^ ((r32 >> 2) & 3));
  unsigned kb1 = lds0 + 2048 * (r32 >> 3) + 64 * (r32 & 7) + 16 * ((2 + hh) ^ ((r32 >> 2) & 3));
  unsigned vb0 = lds0 + 64 * (4 * hh + q4) + 16 * ((2 * blk + (p4 >> 1)) ^ (0 + hh)) + 8 * (p4 & 1);
  unsigned vb1 = lds0 + 2048 + 64 * (4 * hh + q4) + 16 * ((2 * blk + (p4 >> 1)) ^ (2 + hh)) + 8 * (p4 & 1);
  const int eg = w & 1, kh = w >> 1;
  const int pa = (0x78 >> (2 * (r16 >> 2))) & 3;
  const int pg = (0x78 >> (2 * gq)) & 3;
  unsigned keb = lds0 + 2048 * (4 * kh + (pa >> 1)) + 64 * (4 * (pa & 1) + (r16 & 3)) + 16 * (gq ^ pa);
  unsigned veb0 = lds0 + 2048 * (4 * kh + (pg >> 1)) + 64 * (4 * (pg & 1) + q4) + 16 * ((0 + (p4 >> 1)) ^ pg) + 8 * (p4 & 1);
  unsigned veb1 = lds0 + 2048 * (4 * kh + (pg >> 1)) + 64 * (4 * (pg & 1) + q4) + 16 * ((2 + (p4 >> 1)) ^ pg) + 8 * (p4 & 1);

  issue(0);
  issue(1);
  issue(2);
  eissue(0, 0);
  eissue(0, 1);
  eissue(0, 2);

  auto unit = [&](auto U_) __attribute__((always_inline)) {
    constexpr int u = decltype(U_)::value;
    constexpr int n0 = 48 * u;
    STAMP(1);
    bf16x8 qf[8];
    bf16x8 qe[4];
    {
      const int ln = lane_id();
      const int r32 = ln & 31, hh = ln >> 5, q4 = (ln & 15) >> 2, p4 = ln & 3, blk = (ln >> 4) & 1;
      const int sw = ((p4 & 1) << 1) | (p4 >> 1);
      const unsigned wb0 = 2048 * hh + 64 * (0 + q4) + 16 * ((2 * blk + (sw >> 1)) ^ (2 * hh + 0)) + 8 * (sw & 1);
      const unsigned wb1 = 2048 * hh + 64 * (4 + q4) + 16 * ((2 * blk + (sw >> 1)) ^ (2 * hh + 1)) + 8 * (sw & 1);
      unsigned xb[4];
#pragma unroll
      for (int s = 0; s < 4; ++s) xb[s] = 128 * r32 + 16 * ((2 * s + hh) ^ ((r32 >> 1) & 7));
      f32x16 aq[4] = {};
      f32x16 ae = {};
#pragma unroll
      for (int c = 0; c < 8; ++c) {
        if (c == 0) WAIT_BAR(2);
        else if (c <= 5) WAIT_BAR(1);
        else WAIT_BAR(0);
        const lds_cptr sl = L + ((2 * c) & 3) * SLOT;
        const lds_cptr sle = sl + 512 * w;
        const lds_cptr sx = L + ((2 * c + 1) & 3) * SLOT + 4096 * w;
        const lds_cptr sxe = L + XCH + 4096 * (c & 3);
        if (c == 0) prep(n0 + 3);
        else prep(n0 + 2 * c + 2);
#pragma unroll
        for (int s = 0; s < 4; ++s) {
          const bf16x8 xm = ldl(sx + xb[s]);
          const bf16x8 xe = ldl(sxe + xb[s]);
#pragma unroll
          for (int dt = 0; dt < 4; ++dt) {
            bf16x8 a = tr2(sl + wb0 + 4096 * s + 512 * dt, sl + wb1 + 4096 * s + 512 * dt);
            aq[dt] = MFMA32(a, xm, aq[dt]);
            if (c == 0) {
              if (dt == 3) piece(s);
            } else {
              if (s < 2 && (dt & 1)) piece(2 * s + (dt >> 1));
              if (s == 2 && dt == 0) prep(n0 + 2 * c + 3);
              if (s >= 2 && (dt & 1)) piece(2 * (s - 2) + (dt >> 1));
            }
          }
          bf16x8 a = tr2(sle + wb0 + 4096 * s, sle + wb1 + 4096 * s);
          ae = MFMA32(a, xe, ae);
        }
        if (c + 3 < 8) eissue(u, c + 3);
      }
#pragma unroll
      for (int dt = 0; dt < 4; ++dt) {
        aq[dt] *= CSC;
        qf[2 * dt] = pack8(aq[dt][0], aq[dt][1], aq[dt][2], aq[dt][3], aq[dt][4], aq[dt][5], aq[dt][6], aq[dt][7]);
        qf[2 * dt + 1] = pack8(aq[dt][8], aq[dt][9], aq[dt][10], aq[dt][11], aq[dt][12], aq[dt][13], aq[dt][14], aq[dt][15]);
      }
      ae *= CSC;
      bf16x8 e0 = pack8(ae[0], ae[1], ae[2], ae[3], ae[4], ae[5], ae[6], ae[7]);
      bf16x8 e1 = pack8(ae[8], ae[9], ae[10], ae[11], ae[12], ae[13], ae[14], ae[15]);
      *(__attribute__((address_space(3))) bf16x8*)(LW + XCH + 256 * r32 + 16 * ((4 * w + hh) ^ (r32 & 15))) = e0;
      *(__attribute__((address_space(3))) bf16x8*)(LW + XCH + 256 * r32 + 16 * ((4 * w + 2 + hh) ^ (r32 & 15))) = e1;
    }

    STAMP(6);
    f32x16 o[4] = {};
    f32x4 oe[8] = {};
    float m = 0.f, l = 0.f, me = 0.f, le = 0.f;
    f32x16 negm = {};
    f32x4 negme = {};
#pragma unroll 1
    for (int t = 0; t < 16; ++t) {
      STAMP(7);
      if (t == 0) WAIT_BAR(4);
      else WAIT_BAR(8);
      STAMP(0);
      if (t == 0) issue(n0 + 18);
      prep(n0 + 16 + 2 * t + 3);
      if (t == 0) {
        const int ln = lane_id();
        const int r16 = ln & 15, gq = ln >> 4;
#pragma unroll
        for (int s = 0; s < 4; ++s) {
          const int row = 16 * eg + r16;
          qe[s] = ldl(L + XCH + 256 * row + 16 * ((4 * s + gq) ^ (row & 15)));
        }
      }
      const float thr = t == 0 ? -__builtin_inff() : THR_L2;
      const float lo = t == 0 ? -__builtin_inff() : 0.f;
      f32x16 s0, s1;
      f32x4 se0, se1;
      float ma, mb, la = 0.f, lb = 0.f, lf = 0.f;
      u32x4 pw[4];
      u32x4 pwe;
#define EX(x) __builtin_amdgcn_exp2f(x)
#define P2(S, i, DST) do { const float a_ = EX(S[i]), b_ = EX(S[(i) + 1]); la += a_; lb += b_; unsigned d_ = cvtpk(a_, b_); PIN(d_); PIN(la); PIN(lb); DST = d_; } while (0)
#define P2E(S, i, DST) do { const float a_ = EX(S[i]), b_ = EX(S[(i) + 1]); le += a_; lf += b_; unsigned d_ = cvtpk(a_, b_); PIN(d_); PIN(le); PIN(lf); DST = d_; } while (0)
      const lds_cptr k0 = (lds_cptr)(uintptr_t)kb0, k1 = (lds_cptr)(uintptr_t)kb1, ke = (lds_cptr)(uintptr_t)keb;
      bf16x8 kf[16], ef[8];
#define KRD(i) kf[i] = ldl((((i) & 1) ? k1 : k0) + 512 * (((i) & 7) >> 1) + 8192 * ((i) >> 3))
#define ERD(i) ef[i] = ldl(ke + 512 * ((i) >> 1) + 4096 * ((i) & 1))
      KRD(0); KRD(1); KRD(2); KRD(3);
      SB();
#pragma unroll
      for (int i = 0; i < 8; ++i) {
        if (i + 4 < 8) { KRD(i + 4); } else { ERD(i - 4); }
        if (i == 0) s0 = mfma32_head(kf[i], qf[i], negm);
        else s0 = MFMA32(kf[i], qf[i], s0);
        if (i == 2) piece(0);
        if (i == 6) piece(1);
        SB();
      }
#pragma unroll
      for (int i = 0; i < 8; ++i) {
        if (i + 4 < 8) { ERD(i + 4); } else { KRD(i + 4); }
        if (i == 0) se0 = mfma16_head(ef[i], qe[0], negme);
        else if (i == 1) se1 = mfma16_head(ef[i], qe[0], negme);
        else if (i & 1) se1 = MFMA16(ef[i], qe[i >> 1], se1);
        else se0 = MFMA16(ef[i], qe[i >> 1], se0);
        if (i == 3) { ma = MX3(s0[0], s0[1], s0[2]); mb = MX3(s0[3], s0[4], s0[5]); PIN(ma); PIN(mb); }
        if (i == 4) { ma = MX3(ma, s0[6], s0[7]); mb = MX3(mb, s0[8], s0[9]); PIN(ma); PIN(mb); }
        if (i == 5) { ma = MX3(ma, s0[10], s0[11]); mb = MX3(mb, s0[12], s0[13]); PIN(ma); PIN(mb); }
        if (i == 6) { ma = MX3(ma, s0[14], s0[15]); ma = swap32max(__builtin_fmaxf(ma, mb)); PIN(ma); }
        SB();
      }
      {
        const float pm = ma;
        if (__builtin_expect(__any(pm > thr), 0)) {
          const float d = fmaxf(pm, lo);
          const float al = __builtin_amdgcn_exp2f(-fmaxf(d, 0.f));
          m += d;
          l *= al;
#pragma unroll
          for (int c = 0; c < 4; ++c) o[c] *= al;
          s0 -= d;
#pragma unroll
          for (int r = 0; r < 16; ++r) negm[r] = -m;
        }
      }
      SB();
      float pe;
#pragma unroll
      for (int i = 0; i < 8; ++i) {
        if (i + 4 < 8) { KRD(i + 12); }
        if (i == 0) s1 = mfma32_head(kf[8 + i], qf[i], negm);
        else s1 = MFMA32(kf[8 + i], qf[i], s1);
        P2(s0, 2 * i, pw[i >> 2][i & 3]);
        if (i == 1) piece(2);
        if (i == 5) piece(3);
        if (i == 2) { pe = MX3(se0[0], se0[1], se0[2]); pe = MX3(pe, se0[3], se1[0]); PIN(pe); }
        if (i == 3) { pe = MX3(pe, se1[1], se1[2]); pe = __builtin_fmaxf(pe, se1[3]); PIN(pe); }
        if (i == 4) { pe = swap16max(pe); PIN(pe); }
        if (i == 6) { pe = swap32max(pe); PIN(pe); }
        SB();
      }
#undef KRD
#undef ERD
      {
        ma = MX3(s1[0], s1[1], s1[2]); mb = MX3(s1[3], s1[4], s1[5]);
        ma = MX3(ma, s1[6], s1[7]); mb = MX3(mb, s1[8], s1[9]);
        ma = MX3(ma, s1[10], s1[11]); mb = MX3(mb, s1[12], s1[13]);
        ma = MX3(ma, s1[14], s1[15]);
        const float pm = swap32max(__builtin_fmaxf(ma, mb));
        if (__builtin_expect(__any((pm > THR_L2) | (pe > thr)), 0)) {
          const float d = fmaxf(pm, 0.f);
          const float al = __builtin_amdgcn_exp2f(-d);
          m += d;
          l *= al;
          la *= al;
          lb *= al;
#pragma unroll
          for (int c = 0; c < 4; ++c) o[c] *= al;
#pragma unroll
          for (int i = 0; i < 8; ++i) {
            const unsigned v = pw[i >> 2][i & 3];
            pw[i >> 2][i & 3] = cvtpk(__uint_as_float(v << 16) * al, __uint_as_float(v & 0xffff0000u) * al);
          }
          s1 -= d;
#pragma unroll
          for (int r = 0; r < 16; ++r) negm[r] = -m;
          const float de = fmaxf(pe, lo);
          const float ale = __builtin_amdgcn_exp2f(-fmaxf(de, 0.f));
          me += de;
          le *= ale;
#pragma unroll
          for (int c = 0; c < 8; ++c) oe[c] *= ale;
          se0 -= de;
          se1 -= de;
#pragma unroll
          for (int r = 0; r < 4; ++r) negme[r] = -me;
        }
      }
      STAMP(2);
      WAIT_BAR(8);
      STAMP(3);
      prep(n0 + 16 + 2 * t + 4);
      {
        const lds_cptr v0 = (lds_cptr)(uintptr_t)vb0 + SLOT, v1 = (lds_cptr)(uintptr_t)vb1 + SLOT;
        const lds_cptr e0 = (lds_cptr)(uintptr_t)veb0 + SLOT, e1 = (lds_cptr)(uintptr_t)veb1 + SLOT;
        bf16x8 vf[16], wf[8];
#define VRD(i) vf[i] = tr2(v0 + 4096 * ((i) >> 2) + 512 * ((i) & 3), v1 + 4096 * ((i) >> 2) + 512 * ((i) & 3))
#define WRD(i) wf[i] = tr2((((i) & 1) ? e1 : e0) + 512 * ((i) >> 1), (((i) & 1) ? e1 : e0) + 512 * ((i) >> 1) + 4096)
        VRD(0); VRD(1); VRD(2);
        SB();
#pragma unroll
        for (int i = 0; i < 16; ++i) {
          const int s = i >> 2, c = i & 3;
          if (i + 3 < 16) { VRD(i + 3); } else { WRD(i - 13); }
          o[c] = MFMA32(vf[i], __builtin_bit_cast(bf16x8, pw[s]), o[c]);
          if (i < 8) P2(s1, 2 * i, pw[2 + (i >> 2)][i & 3]);
          if (i >= 8 && (i & 1) == 0) { const int j = (i - 8) >> 1; if (j < 2) P2E(se0, 2 * j, pwe[j]); else P2E(se1, 2 * j - 4, pwe[j]); }
          if (c == 1) piece(s);
          SB();
        }
#pragma unroll
        for (int i = 0; i < 8; ++i) {
          if (i + 3 < 8) { WRD(i + 3); }
          oe[i] = MFMA16(wf[i], __builtin_bit_cast(bf16x8, pwe), oe[i]);
          SB();
        }
#undef VRD
#undef WRD
      }
      l += la + lb;
      le += lf;
#undef EX
#undef P2
#undef P2E
      STAMP(5);
      const unsigned dir = (t & 1) ? 0u - 2 * SLOT : 2 * SLOT;
      kb0 += dir; kb1 += dir; keb += dir; vb0 += dir; vb1 += dir; veb0 += dir; veb1 += dir;
      PIN(kb0); PIN(kb1); PIN(keb); PIN(vb0); PIN(vb1); PIN(veb0); PIN(veb1);
    }

    u16* ob = o_ws + (size_t)u * (NB * SQ) * HD + (size_t)(b * SQ) * HD + h * DH;
    const int ln = lane_id();
    {
      const int r32 = ln & 31, hh = ln >> 5;
      l = swap32sum(l);
      const float inv = 1.0f / l;
      u16* orow = ob + (size_t)(32 * w + r32) * HD + 16 * hh;
#pragma unroll
      for (int c = 0; c < 4; ++c) {
        bf16x8 v0 = pack8(o[c][0] * inv, o[c][1] * inv, o[c][2] * inv, o[c][3] * inv, o[c][4] * inv, o[c][5] * inv, o[c][6] * inv, o[c][7] * inv);
        bf16x8 v1 = pack8(o[c][8] * inv, o[c][9] * inv, o[c][10] * inv, o[c][11] * inv, o[c][12] * inv, o[c][13] * inv, o[c][14] * inv, o[c][15] * inv);
        *(bf16x8*)(orow + 32 * c) = v0;
        *(bf16x8*)(orow + 32 * c + 8) = v1;
      }
    }
    {
      const int lane = ln, r16 = ln & 15, gq = ln >> 4;
      le = swap16sum(le);
      le = swap32sum(le);
      if (lane < 16) {
        f32x2 ml = {me, le};
        *(__attribute__((address_space(3))) f32x2*)(LW + XCH + 4096 * (w ^ 2) + 8 * lane) = ml;
      }
      LDS_BAR();
      f32x2 pml = *(const __attribute__((address_space(3))) f32x2*)(L + XCH + 4096 * w + 8 * r16);
      const float M = fmaxf(me, pml[0]);
      const float sc = __builtin_amdgcn_exp2f(me - M), scp = __builtin_amdgcn_exp2f(pml[0] - M);
      const float f = sc / (le * sc + pml[1] * scp);
      asm volatile("s_waitcnt lgkmcnt(0)" ::: "memory");
      f32x4 keep[4];
#pragma unroll
      for (int i = 0; i < 4; ++i) {
        f32x4 snd;
#pragma unroll
        for (int r = 0; r < 4; ++r) {
          snd[r] = (kh ? oe[i][r] : oe[4 + i][r]) * f;
          keep[i][r] = (kh ? oe[4 + i][r] : oe[i][r]) * f;
        }
        *(__attribute__((address_space(3))) f32x4*)(LW + XCH + 4096 * w + 1024 * i + 16 * lane) = snd;
      }
      LDS_BAR();
      u16* erow = ob + (size_t)(128 + 16 * eg + r16) * HD;
#pragma unroll
      for (int i = 0; i < 4; ++i) {
        f32x4 rcv = *(const __attribute__((address_space(3))) f32x4*)(L + XCH + 4096 * (w ^ 2) + 1024 * i + 16 * lane);
        const int dt = 4 * kh + i;
        u32x2 v = {cvtpk(keep[i][0] + rcv[0], keep[i][1] + rcv[1]), cvtpk(keep[i][2] + rcv[2], keep[i][3] + rcv[3])};
        const int p = 32 * (dt >> 1) + 16 * (gq & 1) + 4 * (2 * (dt & 1) + (gq >> 1));
        *(u32x2*)(erow + p) = v;
      }
      if (u == 0) {
        LDS_BAR();
        eissue(1, 0);
        eissue(1, 1);
        eissue(1, 2);
      }
    }
  };
  unit(std::integral_constant<int, 0>{});
  unit(std::integral_constant<int, 1>{});
  asm volatile("s_waitcnt vmcnt(0)" ::: "memory");
}

constexpr unsigned S2 = 28672, NS2 = 5, LDS2 = S2 * NS2;

__global__ __launch_bounds__(256, 1) void oproj_kernel(const u16* __restrict__ o_ws, const u16* __restrict__ woA, const u16* __restrict__ woB,
                                                       u16* __restrict__ out) {
  extern __shared__ __attribute__((aligned(16))) char smem[];
  const int tid = threadIdx.x, lane = tid & 63;
  const int w = __builtin_amdgcn_readfirstlane(tid >> 6);
  const int g = blockIdx.x, xcd = g & 7, idx = g >> 3;
  const int blkk = xcd >> 2, mt = 4 * (xcd & 3) + (idx >> 3), nt8 = idx & 7;
  const unsigned lds0 = (unsigned)(uintptr_t)smem;
  const lds_cptr L = (lds_cptr)smem;
  const lds_ptr LW = (lds_ptr)smem;
  const char* Op = (const char*)(o_ws + (size_t)blkk * (NB * SQ) * HD + (size_t)(mt * 160) * HD);
  const char* Wp = (const char*)((blkk ? woB : woA) + nt8 * 64);
  const int r32 = lane & 31, hh = lane >> 5;
  const int q4 = (lane & 15) >> 2, p4 = lane & 3, blk = (lane >> 4) & 1;
  const unsigned voffO = (unsigned)(8 * w + (lane >> 3)) * (HD * 2) + 16 * ((lane & 7) ^ (4 * (w & 1) + (lane >> 4)));
  const unsigned voffW = (unsigned)(8 * w + ((lane >> 2) & 7)) * (DM * 2) + 64 * (lane >> 5) + 16 * (lane & 3);
  auto issue = [&](int c, int slot) {
    const int cc = c < 64 ? c : 0;
    const unsigned dst = lds0 + (unsigned)slot * S2 + (unsigned)w * 1024;
    const char* so = Op + (size_t)cc * 128;
#pragma unroll
    for (int j = 0; j < 5; ++j) dma16(so + (size_t)j * 32 * (HD * 2), voffO, dst + 4096 * j);
    const char* sw = Wp + (size_t)cc * 64 * (DM * 2);
    dma16(sw, voffW, dst + 20480);
    dma16(sw + 32 * (DM * 2), voffW, dst + 20480 + 4096);
  };
  const unsigned ob = 128 * r32 + 16 * ((2 * w + hh) ^ ((r32 >> 1) & 7));
  const unsigned wb = 20480 + 1024 * (4 * (w >> 1) + 2 * hh) + 64 * (4 * (w & 1) + q4) + 32 * blk + 8 * p4;
  f32x16 acc[2][5] = {};
#pragma unroll
  for (int c = 0; c < 4; ++c) issue(c, c);
  int slot = 0, islot = 4;
#pragma unroll 1
  for (int c = 0; c < 64; ++c) {
    WAIT_BAR(21);
    issue(c + 4, islot);
    const lds_cptr sl = L + slot * S2;
    bf16x8 a0 = tr2(sl + wb, sl + wb + 1024);
    bf16x8 a1 = tr2(sl + wb + 512, sl + wb + 512 + 1024);
#pragma unroll
    for (int i = 0; i < 5; ++i) {
      bf16x8 bb = ldl(sl + ob + 4096 * i);
      acc[0][i] = MFMA32(a0, bb, acc[0][i]);
      acc[1][i] = MFMA32(a1, bb, acc[1][i]);
    }
    slot = slot == 4 ? 0 : slot + 1;
    islot = islot == 4 ? 0 : islot + 1;
  }
  WAIT_BAR(0);
  const int kn = w >> 1;
  {
#pragma unroll
    for (int i = 0; i < 5; ++i)
#pragma unroll
      for (int z = 0; z < 4; ++z) {
        f32x4 v;
#pragma unroll
        for (int r = 0; r < 4; ++r) v[r] = kn ? acc[0][i][4 * z + r] : acc[1][i][4 * z + r];
        *(__attribute__((address_space(3))) f32x4*)(LW + 20480 * w + 4096 * i + 1024 * z + 16 * lane) = v;
      }
  }
  LDS_BAR();
  f32x16 k1[5];
#pragma unroll
  for (int i = 0; i < 5; ++i)
#pragma unroll
    for (int z = 0; z < 4; ++z) {
      f32x4 v = *(const __attribute__((address_space(3))) f32x4*)(L + 20480 * (w ^ 2) + 4096 * i + 1024 * z + 16 * lane);
#pragma unroll
      for (int r = 0; r < 4; ++r) k1[i][4 * z + r] = (kn ? acc[1][i][4 * z + r] : acc[0][i][4 * z + r]) + v[r];
    }
  LDS_BAR();
  const int kz = w & 1;
#pragma unroll
  for (int i = 0; i < 5; ++i)
#pragma unroll
    for (int z = 0; z < 2; ++z) {
      f32x4 v;
#pragma unroll
      for (int r = 0; r < 4; ++r) v[r] = kz ? k1[i][4 * z + r] : k1[i][8 + 4 * z + r];
      *(__attribute__((address_space(3))) f32x4*)(LW + 10240 * w + 2048 * i + 1024 * z + 16 * lane) = v;
    }
  LDS_BAR();
#pragma unroll
  for (int i = 0; i < 5; ++i) {
    u16* orow = out + (size_t)(mt * 160 + 32 * i + r32) * 1024 + blkk * 512 + nt8 * 64 + 32 * kn + 16 * kz + 4 * hh;
#pragma unroll
    for (int z = 0; z < 2; ++z) {
      f32x4 v = *(const __attribute__((address_space(3))) f32x4*)(L + 10240 * (w ^ 1) + 2048 * i + 1024 * z + 16 * lane);
      float f[4];
#pragma unroll
      for (int r = 0; r < 4; ++r) f[r] = (kz ? k1[i][8 + 4 * z + r] : k1[i][4 * z + r]) + v[r];
      u32x2 pv = {cvtpk(f[0], f[1]), cvtpk(f[2], f[3])};
      *(u32x2*)(orow + 8 * z) = pv;
    }
  }
}

extern "C" void kernel_launch(void* const* d_in, const int* in_sizes, int n_in, void* d_out, int out_size, void* d_ws, size_t ws_size,
                              hipStream_t stream) {
  const u16* xA = (const u16*)d_in[0];
  const u16* kA = (const u16*)d_in[1];
  const u16* vA = (const u16*)d_in[2];
  const u16* wqA = (const u16*)d_in[3];
  const u16* woA = (const u16*)d_in[4];
  const u16* xB = (const u16*)d_in[5];
  const u16* kB = (const u16*)d_in[6];
  const u16* vB = (const u16*)d_in[7];
  const u16* wqB = (const u16*)d_in[8];
  const u16* woB = (const u16*)d_in[9];
  u16* o_ws = (u16*)d_ws;
  (void)hipFuncSetAttribute((const void*)attn_kernel, hipFuncAttributeMaxDynamicSharedMemorySize, (int)LDS1);
  (void)hipFuncSetAttribute((const void*)oproj_kernel, hipFuncAttributeMaxDynamicSharedMemorySize, (int)LDS2);
  hipLaunchKernelGGL(attn_kernel, dim3(512), dim3(256), LDS1, stream, xA, kA, vA, wqA, xB, kB, vB, wqB, o_ws);
  hipLaunchKernelGGL(oproj_kernel, dim3(256), dim3(256), LDS2, stream, (const u16*)o_ws, woA, woB, (u16*)d_out);
}
```

```cpp
#define PRIOB 1
#include <hip/hip_runtime.h>
#include <hip/hip_bf16.h>
#include <stdint.h>
#include <type_traits>
#include <utility>

typedef short bf16x8 __attribute__((ext_vector_type(8)));
typedef short s16x4 __attribute__((ext_vector_type(4)));
typedef float f32x16 __attribute__((ext_vector_type(16)));
typedef float f32x4 __attribute__((ext_vector_type(4)));
typedef float f32x2 __attribute__((ext_vector_type(2)));
typedef __bf16 bf16x2_t __attribute__((ext_vector_type(2)));
typedef unsigned u32x4 __attribute__((ext_vector_type(4)));
typedef unsigned u32x2 __attribute__((ext_vector_type(2)));
typedef unsigned short u16;
typedef __attribute__((address_space(3))) char* lds_ptr;
typedef __attribute__((address_space(3))) const char* lds_cptr;
typedef short v4i16_t __attribute__((ext_vector_type(4)));

#define DMAPOS 0
#define KV_AUX " nt"
#define DMA_AUX ""
#define STAMP(i)
#define DI __device__ __forceinline__
#define MFMA32(a, b, c) __builtin_amdgcn_mfma_f32_32x32x16_bf16((a), (b), (c), 0, 0, 0)
#define MFMA16(a, b, c) __builtin_amdgcn_mfma_f32_16x16x32_bf16((a), (b), (c), 0, 0, 0)
#define STR2(x) #x
#define STR(x) STR2(x)
#define WAIT_BAR(N) asm volatile("s_waitcnt vmcnt(" STR(N) ") lgkmcnt(0)\n\ts_barrier" ::: "memory")
#define LDS_BAR() asm volatile("s_waitcnt lgkmcnt(0)\n\ts_barrier" ::: "memory")

constexpr int SQ = 160, SK = 1024, DH = 128, NH = 32, NB = 16, DM = 512, HD = 4096;
constexpr float CSC = 0.088388347648318447f * 1.4426950408889634f;
constexpr float THR_L2 = 8.0f;

DI unsigned cvtpk(float lo, float hi) {
  f32x2 v = {lo, hi};
  bf16x2_t b = __builtin_convertvector(v, bf16x2_t);
  return __builtin_bit_cast(unsigned, b);
}
DI bf16x8 pack8(float a0, float a1, float a2, float a3, float a4, float a5, float a6, float a7) {
  u32x4 w = {cvtpk(a0, a1), cvtpk(a2, a3), cvtpk(a4, a5), cvtpk(a6, a7)};
  return __builtin_bit_cast(bf16x8, w);
}
DI s16x4 trd(lds_cptr p) {
  return __builtin_bit_cast(s16x4, __builtin_amdgcn_ds_read_tr16_b64_v4i16((__attribute__((address_space(3))) v4i16_t*)p));
}
DI bf16x8 tr2(lds_cptr p0, lds_cptr p1) {
  s16x4 a = trd(p0), b = trd(p1);
  return __builtin_shufflevector(a, b, 0, 1, 2, 3, 4, 5, 6, 7);
}
DI bf16x8 ldl(lds_cptr p) { return *(const __attribute__((address_space(3))) bf16x8*)p; }
DI void dma16(const char* sbase, unsigned voff, unsigned lds_dst) {
  asm volatile("s_mov_b32 m0, %2\n\ts_nop 0\n\tglobal_load_lds_dwordx4 %1, %0" DMA_AUX ::"s"(sbase), "v"(voff), "s"(lds_dst) : "memory");
}
DI void dma16s(const char* sbase, unsigned voff, unsigned lds_dst) {
  asm volatile("s_mov_b32 m0, %2\n\ts_nop 0\n\tglobal_load_lds_dwordx4 %1, %0" KV_AUX ::"s"(sbase), "v"(voff), "s"(lds_dst) : "memory");
}
template <int IM, int IMG, bool NT>
DI void bdma(__amdgpu_buffer_rsrc_t rs, unsigned voff, unsigned so, unsigned dst) {
  unsigned tmp;
  if constexpr (NT)
    asm volatile("s_add_u32 m0, %[dst], %[im]\n\ts_add_u32 %[tmp], %[so], %[ig]\n\tbuffer_load_dwordx4 %[v], %[rs], %[tmp] offen nt lds"
                 : [tmp] "=&s"(tmp)
                 : [dst] "s"(dst), [so] "s"(so), [im] "i"(IM), [ig] "i"(IMG), [v] "v"(voff), [rs] "s"(rs)
                 : "memory", "scc");
  else
    asm volatile("s_add_u32 m0, %[dst], %[im]\n\ts_add_u32 %[tmp], %[so], %[ig]\n\tbuffer_load_dwordx4 %[v], %[rs], %[tmp] offen lds"
                 : [tmp] "=&s"(tmp)
                 : [dst] "s"(dst), [so] "s"(so), [im] "i"(IM), [ig] "i"(IMG), [v] "v"(voff), [rs] "s"(rs)
                 : "memory", "scc");
}
DI float swap32max(float x) {
  auto r = __builtin_amdgcn_permlane32_swap(__float_as_uint(x), __float_as_uint(x), false, false);
  return fmaxf(__uint_as_float(r[0]), __uint_as_float(r[1]));
}
DI float swap16max(float x) {
  auto r = __builtin_amdgcn_permlane16_swap(__float_as_uint(x), __float_as_uint(x), false, false);
  return fmaxf(__uint_as_float(r[0]), __uint_as_float(r[1]));
}
DI float swap32sum(float x) {
  auto r = __builtin_amdgcn_permlane32_swap(__float_as_uint(x), __float_as_uint(x), false, false);
  return __uint_as_float(r[0]) + __uint_as_float(r[1]);
}
DI float swap16sum(float x) {
  auto r = __builtin_amdgcn_permlane16_swap(__float_as_uint(x), __float_as_uint(x), false, false);
  return __uint_as_float(r[0]) + __uint_as_float(r[1]);
}

constexpr unsigned SLOT = 16384, XCH = 65536, LDS1 = 81920;
constexpr size_t WS_QPARK = (size_t)56 << 20;
#define SB() __builtin_amdgcn_sched_barrier(0)
#define MX3(a, b, c) __builtin_fmaxf(__builtin_fmaxf((a), (b)), (c))
#define PIN(x) asm volatile("" : "+v"(x))
template <int N, class F, int... I>
DI void static_for_impl(F&& f, std::integer_sequence<int, I...>) { (f(std::integral_constant<int, I>{}), ...); }
template <int N, class F>
DI void static_for(F&& f) { static_for_impl<N>(f, std::make_integer_sequence<int, N>{}); }
DI int lane_id() {
  int l;
  asm volatile("v_mbcnt_lo_u32_b32 %0, -1, 0\n\tv_mbcnt_hi_u32_b32 %0, -1, %0" : "=v"(l));
  return l;
}
DI f32x16 mfma32_head(bf16x8 a, bf16x8 b, const f32x16& c) {
  f32x16 d;
  asm volatile("s_nop 1\n\tv_mfma_f32_32x32x16_bf16 %0, %1, %2, %3" : "=&v"(d) : "v"(a), "v"(b), "v"(c));
  return d;
}
DI f32x4 mfma16_head(bf16x8 a, bf16x8 b, const f32x4& c) {
  f32x4 d;
  asm volatile("s_nop 1\n\tv_mfma_f32_16x16x32_bf16 %0, %1, %2, %3" : "=&v"(d) : "v"(a), "v"(b), "v"(c));
  return d;
}

__global__ __launch_bounds__(256, 2) void attn_kernel(const u16* __restrict__ xA, const u16* __restrict__ kA, const u16* __restrict__ vA,
                                                      const u16* __restrict__ wqA, const u16* __restrict__ xB, const u16* __restrict__ kB,
                                                      const u16* __restrict__ vB, const u16* __restrict__ wqB, u16* __restrict__ o_ws) {
  extern __shared__ __attribute__((aligned(16))) char smem[];
  const int tid = threadIdx.x, lane = tid & 63;
  const int w = __builtin_amdgcn_readfirstlane(tid >> 6);
  const int g = blockIdx.x, xcd = g & 7, idx = g >> 3;
  const int h = 4 * xcd + (idx & 3), b = idx >> 2;
  const unsigned lds0 = (unsigned)(uintptr_t)smem;
  const lds_cptr L = (lds_cptr)smem;
  const lds_ptr LW = (lds_ptr)smem;

  const size_t kvoff = (size_t)(b * NH + h) * SK * DH * 2;
  const char* Kp0 = (const char*)kA + kvoff;
  const char* Vp0 = (const char*)vA + kvoff;
  const char* Kp1 = (const char*)kB + kvoff;
  const char* Vp1 = (const char*)vB + kvoff;
  const char* Wp0 = (const char*)wqA + h * 256;
  const char* Wp1 = (const char*)wqB + h * 256;

  const int r32 = lane & 31, hh = lane >> 5;
  const int r16 = lane & 15, gq = lane >> 4;
  const int q4 = (lane & 15) >> 2, p4 = lane & 3, blk = (lane >> 4) & 1;

  const unsigned rowl = 8 * (w >> 1) + ((lane >> 2) & 7);
  const unsigned chs = 8 * (w & 1) + 4 * (lane >> 5) + ((lane & 3) ^ (2 * (w >> 1) + ((lane >> 4) & 1)));
  const unsigned voffKV = rowl * 256 + chs * 16;

  auto mk_voffE = [&]() {
    const int ln = lane_id();
    return (unsigned)(8 * w + (ln >> 3)) * (DM * 2) + 16 * ((ln & 7) ^ (4 * (w & 1) + (ln >> 4)));
  };
  auto mk_voffX = [&]() {
    const int ln = lane_id();
    return (unsigned)(16 * w + (ln >> 2)) * (DM * 2) + 16 * ((ln & 3) ^ ((ln >> 4) & 3));
  };
  auto mk_voffW = [&]() {
    unsigned v = voffKV;
    asm volatile("" : "+v"(v));
    return ((v & ~255u) << 5) | (v & 255u);
  };
  const char* Xp0 = (const char*)(xA + (size_t)b * SQ * DM);
  const char* Xp1 = (const char*)(xB + (size_t)b * SQ * DM);

  const char *d_src, *d_src2;
  unsigned d_dst;
  bool d_kv;
  int d_nn;
  const bool tb = g >= 256;
  const int w_ = w;
  if (tb) __builtin_amdgcn_s_setprio(PRIOB);
  auto prep = [&](int nn) {
    d_dst = lds0 + (unsigned)(nn & 3) * SLOT + (unsigned)w * 1024;
    const int ph = nn >> 4, j = nn & 15;
    d_nn = nn;
    const bool isq = tb ? (ph < 2 || ph >= 6) : (ph == 0 || ph == 3 || ph >= 6);
    const int un = tb ? (ph == 0 || ph == 4 || ph == 5) : (ph >= 3 && ph < 6);
    const int hf = tb ? (ph & 1) : (ph == 2 || ph == 5);
    d_kv = !isq;
    if (isq) {
      d_src = (un ? Wp1 : Wp0) + (size_t)j * (32 * 8192);
      d_src2 = (un ? Xp1 : Xp0) + 64 * j;
    } else {
      d_src = ((j & 1) ? (un ? Vp1 : Vp0) : (un ? Kp1 : Kp0)) + (size_t)(8 * hf + (j >> 1)) * 16384;
      d_src2 = d_src;
    }
  };
  auto piece = [&](int j) {
    const unsigned dst = d_dst + 4096u * (unsigned)j;
    if (d_kv) dma16s(d_src + 4096 * j, voffKV, dst);
    else if (j < 2) dma16(d_src + (size_t)j * (16 * 8192), mk_voffW(), dst);
    else dma16(d_src2 + (size_t)(j - 2) * (64 * 1024), mk_voffX(), dst);
  };
  auto issue = [&](int nn) {
    prep(nn);
    piece(0);
    piece(1);
    piece(2);
    piece(3);
  };
  auto eissue = [&](int u, int e) { dma16((u ? Xp1 : Xp0) + 128 * 1024 + 128 * e, mk_voffE(), lds0 + XCH + 4096u * (unsigned)(e & 3) + (unsigned)w * 1024); };

  unsigned kb0 = lds0 + 2048 * (r32 >> 3) + 64 * (r32 & 7) + 16 * ((0 + hh) ^ ((r32 >> 2) & 3));
  unsigned kb1 = lds0 + 2048 * (r32 >> 3) + 64 * (r32 & 7) + 16 * ((2 + hh) ^ ((r32 >> 2) & 3));
  unsigned vb0 = lds0 + 64 * (4 * hh + q4) + 16 * ((2 * blk + (p4 >> 1)) ^ (0 + hh)) + 8 * (p4 & 1);
  unsigned vb1 = lds0 + 2048 + 64 * (4 * hh + q4) + 16 * ((2 * blk + (p4 >> 1)) ^ (2 + hh)) + 8 * (p4 & 1);
  const int eg = w & 1, kh = w >> 1;
  const int pa = (0x78 >> (2 * (r16 >> 2))) & 3;
  const int pg = (0x78 >> (2 * gq)) & 3;
  unsigned keb = lds0 + 2048 * (4 * kh + (pa >> 1)) + 64 * (4 * (pa & 1) + (r16 & 3)) + 16 * (gq ^ pa);
  unsigned veb0 = lds0 + 2048 * (4 * kh + (pg >> 1)) + 64 * (4 * (pg & 1) + q4) + 16 * ((0 + (p4 >> 1)) ^ pg) + 8 * (p4 & 1);
  unsigned veb1 = lds0 + 2048 * (4 * kh + (pg >> 1)) + 64 * (4 * (pg & 1) + q4) + 16 * ((2 + (p4 >> 1)) ^ pg) + 8 * (p4 & 1);

  if (g == 0 && tid < 128) {
    unsigned* sy = (unsigned*)((char*)o_ws + (size_t)2 * NB * SQ * HD * 2) + tid * 32;
    sy[0] = 0u;
    sy[16] = 0u;
  }
  {
    const int u0 = tb ? 1 : 0;
    eissue(u0, 0);
    eissue(u0, 1);
    eissue(u0, 2);
  }
  issue(0);
  issue(1);
  issue(2);

  bf16x8 qf[8];
  bf16x8 qe[4];
  char* qpark = (char*)o_ws + WS_QPARK + (size_t)(g & 255) * 40960;

  auto qphase = [&](const int u, const int n0, const bool park) __attribute__((always_inline)) {
    STAMP(1);
    {
      const int ln = lane_id();
      const int r32 = ln & 31, hh = ln >> 5, q4 = (ln & 15) >> 2, p4 = ln & 3, blk = (ln >> 4) & 1;
      const int sw = ((p4 & 1) << 1) | (p4 >> 1);
      const unsigned wb0 = 2048 * hh + 64 * (0 + q4) + 16 * ((2 * blk + (sw >> 1)) ^ (2 * hh + 0)) + 8 * (sw & 1);
      const unsigned wb1 = 2048 * hh + 64 * (4 + q4) + 16 * ((2 * blk + (sw >> 1)) ^ (2 * hh + 1)) + 8 * (sw & 1);
      const unsigned xb0 = 8192 + 64 * (32 * w + r32) + 16 * ((0 + hh) ^ ((r32 >> 2) & 3));
      const unsigned xb1 = 8192 + 64 * (32 * w + r32) + 16 * ((2 + hh) ^ ((r32 >> 2) & 3));
      unsigned xe_b[4];
#pragma unroll
      for (int s = 0; s < 4; ++s) xe_b[s] = XCH + 128 * r32 + 16 * ((2 * s + hh) ^ ((r32 >> 1) & 7));
      const unsigned we0 = wb0 + 512 * w, we1 = wb1 + 512 * w;
      f32x16 aq[4] = {};
      f32x16 ae = {};
      const __amdgpu_buffer_rsrc_t rsW = __builtin_amdgcn_make_buffer_rsrc((void*)(u ? Wp1 : Wp0), (short)0, DM * HD * 2, 0x00020000);
      const __amdgpu_buffer_rsrc_t rsX = __builtin_amdgcn_make_buffer_rsrc((void*)(u ? Xp1 : Xp0), (short)0, SQ * DM * 2, 0x00020000);
      const unsigned vW = mk_voffW(), vX = mk_voffX();
      const unsigned qdst = lds0 + (unsigned)w * 1024;
      auto qpiece = [&](auto C_, auto J_) __attribute__((always_inline)) {
        constexpr int cn = decltype(C_)::value, j = decltype(J_)::value;
        if constexpr (cn < 16) {
          if constexpr (j < 2) bdma<(cn & 3) * SLOT + 4096 * j, cn * (32 * 8192) + j * (16 * 8192), false>(rsW, vW, 0u, qdst);
          else bdma<(cn & 3) * SLOT + 4096 * j, 64 * cn + (j - 2) * 65536, false>(rsX, vX, 0u, qdst);
        } else {
          piece(j);
        }
      };
      static_for<16>([&](auto C_) __attribute__((always_inline)) {
        constexpr int c = decltype(C_)::value;
        WAIT_BAR(8);
        const lds_cptr sl = L + (c & 3) * SLOT;
        const lds_cptr sxe = L + 4096 * ((c >> 1) & 3);
        if constexpr (c + 3 >= 16) prep(n0 + c + 3);
        bf16x8 xm[2], xe[2], af[10];
        xm[0] = ldl(sl + xb0);
        af[0] = tr2(sl + wb0, sl + wb1);
        af[1] = tr2(sl + wb0 + 512, sl + wb1 + 512);
        xe[0] = ldl(sxe + xe_b[2 * (c & 1)]);
        SB();
#pragma unroll
        for (int i = 0; i < 10; ++i) {
          const int s = i / 5, dt = i % 5;
          const int i2 = i + 2, s2 = i2 / 5, d2 = i2 % 5;
          if (i2 < 10) {
            if (d2 < 4) af[i2] = tr2(sl + wb0 + 4096 * s2 + 512 * d2, sl + wb1 + 4096 * s2 + 512 * d2);
            else af[i2] = tr2(sl + we0 + 4096 * s2, sl + we1 + 4096 * s2);
          }
          if (i == 2) xm[1] = ldl(sl + xb1);
          if (i == 3) xe[1] = ldl(sxe + xe_b[2 * (c & 1) + 1]);
          if (dt < 4) aq[dt] = MFMA32(af[i], xm[s], aq[dt]);
          else ae = MFMA32(af[i], xe[s], ae);
          if (i == 1) qpiece(std::integral_constant<int, c + 3>{}, std::integral_constant<int, 0>{});
          if (i == 3) qpiece(std::integral_constant<int, c + 3>{}, std::integral_constant<int, 1>{});
          if (i == 5) qpiece(std::integral_constant<int, c + 3>{}, std::integral_constant<int, 2>{});
          if (i == 7) qpiece(std::integral_constant<int, c + 3>{}, std::integral_constant<int, 3>{});
          SB();
        }
        if ((c & 1) == 0) {
          if ((c >> 1) + 3 < 8) eissue(u, (c >> 1) + 3);
          else if (park) eissue(0, (c >> 1) + 3 - 8);
        }
      });
#pragma unroll
      for (int dt = 0; dt < 4; ++dt) {
        aq[dt] *= CSC;
        qf[2 * dt] = pack8(aq[dt][0], aq[dt][1], aq[dt][2], aq[dt][3], aq[dt][4], aq[dt][5], aq[dt][6], aq[dt][7]);
        qf[2 * dt + 1] = pack8(aq[dt][8], aq[dt][9], aq[dt][10], aq[dt][11], aq[dt][12], aq[dt][13], aq[dt][14], aq[dt][15]);
      }
      ae *= CSC;
      bf16x8 e0 = pack8(ae[0], ae[1], ae[2], ae[3], ae[4], ae[5], ae[6], ae[7]);
      bf16x8 e1 = pack8(ae[8], ae[9], ae[10], ae[11], ae[12], ae[13], ae[14], ae[15]);
      if (park) {
#pragma unroll
        for (int i = 0; i < 8; ++i) *(bf16x8*)(qpark + w * 8192 + 1024 * i + 16 * ln) = qf[i];
        *(bf16x8*)(qpark + 32768 + 256 * r32 + 16 * (4 * w + hh)) = e0;
        *(bf16x8*)(qpark + 32768 + 256 * r32 + 16 * (4 * w + 2 + hh)) = e1;
      } else {
        *(__attribute__((address_space(3))) bf16x8*)(LW + XCH + 256 * r32 + 16 * ((4 * w + hh) ^ (r32 & 15))) = e0;
        *(__attribute__((address_space(3))) bf16x8*)(LW + XCH + 256 * r32 + 16 * ((4 * w + 2 + hh) ^ (r32 & 15))) = e1;
        LDS_BAR();
        const int row = 16 * eg + (ln & 15);
#pragma unroll
        for (int s = 0; s < 4; ++s) qe[s] = ldl(L + XCH + 256 * row + 16 * ((4 * s + (ln >> 4)) ^ (row & 15)));
      }
    }
    STAMP(6);
  };

  auto attention = [&](auto U_, const int nk, const bool qe_lds) __attribute__((always_inline)) {
    constexpr int u = decltype(U_)::value;
    f32x16 o[4] = {};
    f32x4 oe[8] = {};
    float l = 0.f, le = 0.f;
    f32x16 negm = {};
    f32x4 negme = {};
    const __amdgpu_buffer_rsrc_t rsK = __builtin_amdgcn_make_buffer_rsrc((void*)(u ? Kp1 : Kp0), (short)0, SK * DH * 2, 0x00020000);
    const __amdgpu_buffer_rsrc_t rsV = __builtin_amdgcn_make_buffer_rsrc((void*)(u ? Vp1 : Vp0), (short)0, SK * DH * 2, 0x00020000);
    auto tile = [&](const int t, auto F_) __attribute__((always_inline)) {
      constexpr bool FAST = decltype(F_)::value;
      const unsigned so = (unsigned)t * 16384u;
      const unsigned fdst = lds0 + (unsigned)w * 1024 + ((unsigned)(t & 1) << 15);
      const unsigned vdst = lds0 + (unsigned)w * 1024 + 3 * SLOT - ((unsigned)(t & 1) << 15);
#define PIECE_V(j) do { if constexpr (FAST) bdma<4096 * (j), 16384 + 4096 * (j), true>(rsV, voffKV, so, vdst); else piece(j); } while (0)
#define PIECE_K(j) do { if constexpr (FAST) bdma<4096 * (j), 32768 + 4096 * (j), true>(rsK, voffKV, so, fdst); else piece(j); } while (0)
      STAMP(7);
      WAIT_BAR(8);
      STAMP(0);
      if constexpr (!FAST) {
        if (u == 0 && t == 14 && !tb) {
          eissue(1, 0);
          eissue(1, 1);
          eissue(1, 2);
        }
        prep(nk + 2 * t + 3);
      }
      const float thr = t == 0 ? -__builtin_inff() : THR_L2;
      const float lo = t == 0 ? -__builtin_inff() : 0.f;
      f32x16 s0, s1;
      f32x4 se0, se1;
      float ma, mb, la = 0.f, lb = 0.f, lf = 0.f;
      u32x4 pw[4];
      u32x4 pwe;
#define EX(x) __builtin_amdgcn_exp2f(x)
#define P2(S, i, DST) do { const float a_ = EX(S[i]), b_ = EX(S[(i) + 1]); la += a_; lb += b_; unsigned d_ = cvtpk(a_, b_); PIN(d_); PIN(la); PIN(lb); DST = d_; } while (0)
#define P2E(S, i, DST) do { const float a_ = EX(S[i]), b_ = EX(S[(i) + 1]); le += a_; lf += b_; unsigned d_ = cvtpk(a_, b_); PIN(d_); PIN(le); PIN(lf); DST = d_; } while (0)
      const lds_cptr k0 = (lds_cptr)(uintptr_t)kb0, k1 = (lds_cptr)(uintptr_t)kb1, ke = (lds_cptr)(uintptr_t)keb;
      bf16x8 kf[16], ef[8];
#define KRD(i) kf[i] = ldl((((i) & 1) ? k1 : k0) + 512 * (((i) & 7) >> 1) + 8192 * ((i) >> 3))
#define ERD(i) ef[i] = ldl(ke + 512 * ((i) >> 1) + 4096 * ((i) & 1))
      KRD(0); KRD(1); KRD(2); KRD(3);
      SB();
#pragma unroll
      for (int i = 0; i < 8; ++i) {
        if (i + 4 < 8) { KRD(i + 4); } else { ERD(i - 4); }
        if (i == 0) s0 = mfma32_head(kf[i], qf[i], negm);
        else s0 = MFMA32(kf[i], qf[i], s0);
        if (i == 2) PIECE_V(0);
        if (i == 6) PIECE_V(1);
        SB();
      }
#pragma unroll
      for (int i = 0; i < 8; ++i) {
        if (i + 4 < 8) { ERD(i + 4); } else { KRD(i + 4); }
        if (i == 0) se0 = mfma16_head(ef[i], qe[0], negme);
        else if (i == 1) se1 = mfma16_head(ef[i], qe[0], negme);
        else if (i & 1) se1 = MFMA16(ef[i], qe[i >> 1], se1);
        else se0 = MFMA16(ef[i], qe[i >> 1], se0);
        if (i == 3) { ma = MX3(s0[0], s0[1], s0[2]); mb = MX3(s0[3], s0[4], s0[5]); PIN(ma); PIN(mb); }
        if (i == 4) { ma = MX3(ma, s0[6], s0[7]); mb = MX3(mb, s0[8], s0[9]); PIN(ma); PIN(mb); }
        if (i == 5) { ma = MX3(ma, s0[10], s0[11]); mb = MX3(mb, s0[12], s0[13]); PIN(ma); PIN(mb); }
        if (i == 6) { ma = MX3(ma, s0[14], s0[15]); ma = swap32max(__builtin_fmaxf(ma, mb)); PIN(ma); }
        SB();
      }
      {
        const float pm = ma;
        if (__builtin_expect(__any(pm > thr), 0)) {
          const float d = fmaxf(pm, lo);
          const float al = __builtin_amdgcn_exp2f(-fmaxf(d, 0.f));
          l *= al;
#pragma unroll
          for (int c = 0; c < 4; ++c) o[c] *= al;
          s0 -= d;
          negm -= d;
        }
      }
      SB();
      float pe;
#pragma unroll
      for (int i = 0; i < 8; ++i) {
        if (i + 4 < 8) { KRD(i + 12); }
        if (i == 0) s1 = mfma32_head(kf[8 + i], qf[i], negm);
        else s1 = MFMA32(kf[8 + i], qf[i], s1);
        P2(s0, 2 * i, pw[i >> 2][i & 3]);
        if (i == 1) PIECE_V(2);
        if (i == 5) PIECE_V(3);
        if (i == 2) { pe = MX3(se0[0], se0[1], se0[2]); pe = MX3(pe, se0[3], se1[0]); PIN(pe); }
        if (i == 3) { pe = MX3(pe, se1[1], se1[2]); pe = __builtin_fmaxf(pe, se1[3]); PIN(pe); }
        if (i == 4) { pe = swap16max(pe); PIN(pe); }
        if (i == 6) { pe = swap32max(pe); PIN(pe); }
        SB();
      }
#undef KRD
#undef ERD
      {
        ma = MX3(s1[0], s1[1], s1[2]); mb = MX3(s1[3], s1[4], s1[5]);
        ma = MX3(ma, s1[6], s1[7]); mb = MX3(mb, s1[8], s1[9]);
        ma = MX3(ma, s1[10], s1[11]); mb = MX3(mb, s1[12], s1[13]);
        ma = MX3(ma, s1[14], s1[15]);
        const float pm = swap32max(__builtin_fmaxf(ma, mb));
        if (__builtin_expect(__any((pm > THR_L2) | (pe > thr)), 0)) {
          const float d = fmaxf(pm, 0.f);
          const float al = __builtin_amdgcn_exp2f(-d);
          l *= al;
          la *= al;
          lb *= al;
#pragma unroll
          for (int c = 0; c < 4; ++c) o[c] *= al;
#pragma unroll
          for (int i = 0; i < 8; ++i) {
            const unsigned v = pw[i >> 2][i & 3];
            pw[i >> 2][i & 3] = cvtpk(__uint_as_float(v << 16) * al, __uint_as_float(v & 0xffff0000u) * al);
          }
          s1 -= d;
          negm -= d;
          const float de = fmaxf(pe, lo);
          const float ale = __builtin_amdgcn_exp2f(-fmaxf(de, 0.f));
          le *= ale;
#pragma unroll
          for (int c = 0; c < 8; ++c) oe[c] *= ale;
          se0 -= de;
          se1 -= de;
          negme -= de;
        }
      }
      STAMP(2);
      WAIT_BAR(8);
      STAMP(3);
      if constexpr (!FAST) prep(nk + 2 * t + 4);
      {
        const lds_cptr v0 = (lds_cptr)(uintptr_t)vb0 + SLOT, v1 = (lds_cptr)(uintptr_t)vb1 + SLOT;
        const lds_cptr e0 = (lds_cptr)(uintptr_t)veb0 + SLOT, e1 = (lds_cptr)(uintptr_t)veb1 + SLOT;
        bf16x8 vf[16], wf[8];
#define VRD(i) vf[i] = tr2(v0 + 4096 * ((i) >> 2) + 512 * ((i) & 3), v1 + 4096 * ((i) >> 2) + 512 * ((i) & 3))
#define WRD(i) wf[i] = tr2((((i) & 1) ? e1 : e0) + 512 * ((i) >> 1), (((i) & 1) ? e1 : e0) + 512 * ((i) >> 1) + 4096)
        VRD(0); VRD(1); VRD(2);
        SB();
#pragma unroll
        for (int i = 0; i < 16; ++i) {
          const int s = i >> 2, c = i & 3;
          if (i + 3 < 16) { VRD(i + 3); } else { WRD(i - 13); }
          o[c] = MFMA32(vf[i], __builtin_bit_cast(bf16x8, pw[s]), o[c]);
          if (i < 8) P2(s1, 2 * i, pw[2 + (i >> 2)][i & 3]);
          if (i >= 8 && (i & 1) == 0) { const int j = (i - 8) >> 1; if (j < 2) P2E(se0, 2 * j, pwe[j]); else P2E(se1, 2 * j - 4, pwe[j]); }
          if (i == 1) PIECE_K(0);
          if (i == 5) PIECE_K(1);
          if (i == 9) PIECE_K(2);
          if (i == 13) PIECE_K(3);
          SB();
        }
#pragma unroll
        for (int i = 0; i < 8; ++i) {
          if (i + 3 < 8) { WRD(i + 3); }
          oe[i] = MFMA16(wf[i], __builtin_bit_cast(bf16x8, pwe), oe[i]);
          SB();
        }
#undef VRD
#undef WRD
      }
      l += la + lb;
      le += lf;
#undef EX
#undef P2
#undef P2E
      STAMP(5);
      const unsigned dir = (t & 1) ? 0u - 2 * SLOT : 2 * SLOT;
      kb0 += dir; kb1 += dir; keb += dir; vb0 += dir; vb1 += dir; veb0 += dir; veb1 += dir;
      PIN(kb0); PIN(kb1); PIN(keb); PIN(vb0); PIN(vb1); PIN(veb0); PIN(veb1);
#undef PIECE_V
#undef PIECE_K
    };
#pragma unroll 1
    for (int t = 0; t < 14; ++t) tile(t, std::true_type{});
#pragma unroll 1
    for (int t = 14; t < 16; ++t) tile(t, std::false_type{});

    char* ob = (char*)o_ws + (size_t)((u * NB + b) * NH + h) * (40 * 1024);
    const int ln = lane_id();
    int w = w_;
    asm volatile("" : "+s"(w));
    const int eg = w & 1, kh = w >> 1;
    {
      l = swap32sum(l);
      const float inv = 1.0f / l;
      char* of = ob + w * 8192 + 16 * ln;
#pragma unroll
      for (int c = 0; c < 4; ++c) {
        bf16x8 v0 = pack8(o[c][0] * inv, o[c][1] * inv, o[c][2] * inv, o[c][3] * inv, o[c][4] * inv, o[c][5] * inv, o[c][6] * inv, o[c][7] * inv);
        bf16x8 v1 = pack8(o[c][8] * inv, o[c][9] * inv, o[c][10] * inv, o[c][11] * inv, o[c][12] * inv, o[c][13] * inv, o[c][14] * inv, o[c][15] * inv);
        *(bf16x8*)(of + 2048 * c) = v0;
        *(bf16x8*)(of + 2048 * c + 1024) = v1;
      }
    }
    {
      constexpr unsigned EX0 = 3 * SLOT;
      const int lane = ln, r16 = ln & 15, gq = ln >> 4;
      const float me = -negme[0];
      le = swap16sum(le);
      le = swap32sum(le);
      LDS_BAR();
      if (lane < 16) {
        f32x2 ml = {me, le};
        *(__attribute__((address_space(3))) f32x2*)(LW + EX0 + 4096 * (w ^ 2) + 8 * lane) = ml;
      }
      LDS_BAR();
      f32x2 pml = *(const __attribute__((address_space(3))) f32x2*)(L + EX0 + 4096 * w + 8 * r16);
      const float M = fmaxf(me, pml[0]);
      const float sc = __builtin_amdgcn_exp2f(me - M), scp = __builtin_amdgcn_exp2f(pml[0] - M);
      const float f = sc / (le * sc + pml[1] * scp);
      asm volatile("s_waitcnt lgkmcnt(0)" ::: "memory");
      f32x4 keep[4];
#pragma unroll
      for (int i = 0; i < 4; ++i) {
        f32x4 snd;
#pragma unroll
        for (int r = 0; r < 4; ++r) {
          snd[r] = (kh ? oe[i][r] : oe[4 + i][r]) * f;
          keep[i][r] = (kh ? oe[4 + i][r] : oe[i][r]) * f;
        }
        *(__attribute__((address_space(3))) f32x4*)(LW + EX0 + 4096 * w + 1024 * i + 16 * lane) = snd;
      }
      LDS_BAR();
      char* ef = ob + 4 * 8192 + 4096 * kh + 16 * (32 * (gq & 1) + 16 * eg + r16) + 8 * (gq >> 1);
#pragma unroll
      for (int i = 0; i < 4; ++i) {
        f32x4 rcv = *(const __attribute__((address_space(3))) f32x4*)(L + EX0 + 4096 * (w ^ 2) + 1024 * i + 16 * lane);
        u32x2 v = {cvtpk(keep[i][0] + rcv[0], keep[i][1] + rcv[1]), cvtpk(keep[i][2] + rcv[2], keep[i][3] + rcv[3])};
        *(u32x2*)(ef + 1024 * i) = v;
      }
    }
  };
#pragma unroll 1
  for (int i = 0; i < (tb ? 2 : 1); ++i) qphase((tb && i == 0) ? 1 : 0, 16 * i, tb && i == 0);
  attention(std::integral_constant<int, 0>{}, tb ? 32 : 16, true);
  if (tb) {
    const int ln = lane_id();
#pragma unroll
    for (int i = 0; i < 8; ++i) qf[i] = *(const bf16x8*)(qpark + w * 8192 + 1024 * i + 16 * ln);
#pragma unroll
    for (int s = 0; s < 4; ++s) qe[s] = *(const bf16x8*)(qpark + 32768 + 256 * (16 * eg + (ln & 15)) + 16 * (4 * s + (ln >> 4)));
#pragma unroll
    for (int i = 0; i < 8; ++i) PIN(qf[i]);
#pragma unroll
    for (int s = 0; s < 4; ++s) PIN(qe[s]);
  } else {
    qphase(1, 48, false);
  }
  attention(std::integral_constant<int, 1>{}, 64, !tb);
  asm volatile("s_waitcnt vmcnt(0)" ::: "memory");
}

constexpr unsigned S2 = 36864, NS2 = 4, LDS2 = S2 * NS2;
constexpr size_t WS_SYNC = (size_t)2 * NB * SQ * HD * 2;
constexpr size_t WS_PART = WS_SYNC + 128 * 128;

__global__ __launch_bounds__(512, 2) void oproj_kernel(char* __restrict__ ws, const u16* __restrict__ woA, const u16* __restrict__ woB,
                                                       u16* __restrict__ out) {
  extern __shared__ __attribute__((aligned(16))) char smem[];
  const u16* o_ws = (const u16*)ws;
  const int tid = threadIdx.x, lane = tid & 63;
  const int w = __builtin_amdgcn_readfirstlane(tid >> 6);
  const int wn = w & 3, wk = w >> 2;
  const int g = blockIdx.x, xcd = g & 7, idx = g >> 3;
  const int blkk = xcd >> 2, mt = 4 * (xcd & 3) + (idx >> 3), nt = (idx >> 1) & 3, kh = idx & 1;
  const int tile = (blkk * 16 + mt) * 4 + nt;
  const unsigned lds0 = (unsigned)(uintptr_t)smem;
  const lds_cptr L = (lds_cptr)smem;
  const lds_ptr LW = (lds_ptr)smem;
  const int r32 = lane & 31, hh = lane >> 5;
  const int q4 = (lane & 15) >> 2, p4 = lane & 3, blk = (lane >> 4) & 1;
  const char* src0;
  unsigned voff, pstr, cstr, dst0;
  if (wk == 0) {
    src0 = (const char*)o_ws + (size_t)((blkk * NB + mt) * NH + kh * 16) * (40 * 1024) + 1024 * wn;
    voff = 16 * lane;
    pstr = 8192;
    cstr = 0;
    dst0 = lds0 + (unsigned)wn * 1024;
  } else {
    src0 = (const char*)((blkk ? woB : woA) + (size_t)(kh * 2048) * DM + nt * 128);
    voff = (unsigned)(8 * (wn >> 1) + ((lane >> 2) & 7)) * (DM * 2) + 128 * (wn & 1) + 64 * (lane >> 5) + 16 * (lane & 3);
    pstr = 16 * (DM * 2);
    cstr = 64 * (DM * 2);
    dst0 = lds0 + 20480 + (unsigned)wn * 1024;
  }
#define STAG 2
  const int coff = STAG * (nt + (mt & 3));
  auto piece = [&](int c, int j) {
    const int cc = (c + coff) & 31;
    const unsigned co = wk == 0 ? (unsigned)(cc >> 1) * 40960u + (unsigned)(cc & 1) * 4096u : (unsigned)cc * cstr;
    dma16(src0 + (size_t)co + (size_t)((unsigned)j * pstr), voff, dst0 + (unsigned)(c & 3) * S2 + 4096u * (unsigned)j);
  };
  const unsigned ob0 = 2048 * wk + 16 * lane;
  const unsigned wb = 20480 + 8192 * wk + 512 * wn + 64 * (4 * hh + q4) + 32 * blk + 8 * p4;
#define DUMP()
  f32x16 acc[5] = {};
  bf16x8 fa[2][2], fb[2][2][5];
  auto rdA = [&](int buf, const lds_cptr sl, int s2) { fa[buf][s2] = tr2(sl + wb + 4096 * s2, sl + wb + 4096 * s2 + 2048); };
  auto rdB = [&](int buf, const lds_cptr sl, int s2, int i) { fb[buf][s2][i] = ldl(sl + ob0 + 1024 * s2 + 4096 * i); };
#pragma unroll
  for (int c = 0; c < 4; ++c) {
#pragma unroll
    for (int j = 0; j < 4; ++j) piece(c, j);
    if (wk == 0) piece(c, 4);
  }
  if (wk == 0) WAIT_BAR(15);
  else WAIT_BAR(12);
#pragma unroll
  for (int s2 = 0; s2 < 2; ++s2) {
    rdA(0, L, s2);
#pragma unroll
    for (int i = 0; i < 5; ++i) rdB(0, L, s2, i);
  }
  auto step = [&](int c, auto B_) __attribute__((always_inline)) {
    constexpr int bf = decltype(B_)::value;
    if (wk == 0) WAIT_BAR(10);
    else WAIT_BAR(8);
    const lds_cptr sl = L + ((c + 1) & 3) * S2;
    SB();
#pragma unroll
    for (int s2 = 0; s2 < 2; ++s2) {
#pragma unroll
      for (int i = 0; i < 5; ++i) {
        if (i == 0) rdA(bf ^ 1, sl, s2);
        rdB(bf ^ 1, sl, s2, i);
        acc[i] = MFMA32(fa[bf][s2], fb[bf][s2][i], acc[i]);
        const int k = 5 * s2 + i;
        if ((k & 1) == 1 && k < 8) piece(c + 4, k >> 1);
        if (k == 9 && wk == 0) piece(c + 4, 4);
        SB();
      }
    }
  };
  STAMP(0);
#pragma unroll 1
  for (int c = 0; c < 32; c += 2) {
    step(c, std::integral_constant<int, 0>{});
    step(c + 1, std::integral_constant<int, 1>{});
  }
  STAMP(1);
  WAIT_BAR(0);
#pragma unroll
  for (int i = 0; i < 5; ++i)
#pragma unroll
    for (int z = 0; z < 2; ++z) {
      f32x4 v;
#pragma unroll
      for (int r = 0; r < 4; ++r) v[r] = wk ? acc[i][4 * z + r] : acc[i][8 + 4 * z + r];
      *(__attribute__((address_space(3))) f32x4*)(LW + 10240 * w + 2048 * i + 1024 * z + 16 * lane) = v;
    }
  LDS_BAR();
  f32x4 res[5][2];
#pragma unroll
  for (int i = 0; i < 5; ++i)
#pragma unroll
    for (int z = 0; z < 2; ++z) {
      f32x4 v = *(const __attribute__((address_space(3))) f32x4*)(L + 10240 * (w ^ 4) + 2048 * i + 1024 * z + 16 * lane);
#pragma unroll
      for (int r = 0; r < 4; ++r) res[i][z][r] = (wk ? acc[i][8 + 4 * z + r] : acc[i][4 * z + r]) + v[r];
    }
  STAMP(2);
  unsigned* sync = (unsigned*)(ws + WS_SYNC + (size_t)tile * 128);
  volatile __attribute__((address_space(3))) unsigned* tk = (volatile __attribute__((address_space(3))) unsigned*)(LW + 90112);
  if (tid == 0) *tk = __hip_atomic_fetch_add(sync, 1u, __ATOMIC_RELAXED, __HIP_MEMORY_SCOPE_AGENT);
  LDS_BAR();
  const unsigned ticket = __builtin_amdgcn_readfirstlane(*tk);
  const __amdgpu_buffer_rsrc_t rs = __builtin_amdgcn_make_buffer_rsrc(ws + WS_PART + (size_t)tile * 81920, 0, 81920, 0x00020000);
  const unsigned poff = (unsigned)w * 10240 + 16 * lane;
  if (ticket == 0) {
#pragma unroll
    for (int i = 0; i < 5; ++i)
#pragma unroll
      for (int z = 0; z < 2; ++z)
        __builtin_amdgcn_raw_buffer_store_b128(__builtin_bit_cast(u32x4, res[i][z]), rs, poff + 2048 * i + 1024 * z, 0, 16);
    asm volatile("s_waitcnt vmcnt(0)" ::: "memory");
    __builtin_amdgcn_s_barrier();
    if (tid == 0) __hip_atomic_store(sync + 16, 1u, __ATOMIC_RELAXED, __HIP_MEMORY_SCOPE_AGENT);
    STAMP(3);
    DUMP();
    return;
  }
  if (tid == 0) {
    while (__hip_atomic_load(sync + 16, __ATOMIC_RELAXED, __HIP_MEMORY_SCOPE_AGENT) == 0u) __builtin_amdgcn_s_sleep(1);
    __builtin_amdgcn_fence(__ATOMIC_ACQUIRE, "agent");
    asm volatile("s_waitcnt vmcnt(0)" ::: "memory");
  }
  __syncthreads();
  STAMP(4);
#pragma unroll
  for (int i = 0; i < 5; ++i) {
    u16* orow = out + (size_t)(mt * 160 + 32 * i + r32) * 1024 + blkk * 512 + nt * 128 + 32 * wn + 16 * wk + 4 * hh;
#pragma unroll
    for (int z = 0; z < 2; ++z) {
      const f32x4 v = __builtin_bit_cast(f32x4, __builtin_amdgcn_raw_buffer_load_b128(rs, poff + 2048 * i + 1024 * z, 0, 16));
      u32x2 pv = {cvtpk(res[i][z][0] + v[0], res[i][z][1] + v[1]), cvtpk(res[i][z][2] + v[2], res[i][z][3] + v[3])};
      *(u32x2*)(orow + 8 * z) = pv;
    }
  }
  STAMP(5);
  DUMP();
}

extern "C" void kernel_launch(void* const* d_in, const int* in_sizes, int n_in, void* d_out, int out_size, void* d_ws, size_t ws_size,
                              hipStream_t stream) {
  const u16* xA = (const u16*)d_in[0];
  const u16* kA = (const u16*)d_in[1];
  const u16* vA = (const u16*)d_in[2];
  const u16* wqA = (const u16*)d_in[3];
  const u16* woA = (const u16*)d_in[4];
  const u16* xB = (const u16*)d_in[5];
  const u16* kB = (const u16*)d_in[6];
  const u16* vB = (const u16*)d_in[7];
  const u16* wqB = (const u16*)d_in[8];
  const u16* woB = (const u16*)d_in[9];
  u16* o_ws = (u16*)d_ws;
  (void)hipFuncSetAttribute((const void*)attn_kernel, hipFuncAttributeMaxDynamicSharedMemorySize, (int)LDS1);
  (void)hipFuncSetAttribute((const void*)oproj_kernel, hipFuncAttributeMaxDynamicSharedMemorySize, (int)LDS2);
  hipLaunchKernelGGL(attn_kernel, dim3(512), dim3(256), LDS1, stream, xA, kA, vA, wqA, xB, kB, vB, wqB, o_ws);
  hipLaunchKernelGGL(oproj_kernel, dim3(256), dim3(512), LDS2, stream, (char*)d_ws, woA, woB, (u16*)d_out);
}
```

```cpp
#include <hip/hip_runtime.h>
#include <hip/hip_bf16.h>
#include <stdint.h>
#include <type_traits>
#include <utility>

typedef short bf16x8 __attribute__((ext_vector_type(8)));
typedef short s16x4 __attribute__((ext_vector_type(4)));
typedef float f32x16 __attribute__((ext_vector_type(16)));
typedef float f32x4 __attribute__((ext_vector_type(4)));
typedef float f32x2 __attribute__((ext_vector_type(2)));
typedef __bf16 bf16x2_t __attribute__((ext_vector_type(2)));
typedef unsigned u32x4 __attribute__((ext_vector_type(4)));
typedef unsigned u32x2 __attribute__((ext_vector_type(2)));
typedef unsigned short u16;
typedef __attribute__((address_space(3))) char* lds_ptr;
typedef __attribute__((address_space(3))) const char* lds_cptr;
typedef short v4i16_t __attribute__((ext_vector_type(4)));

#define DMAPOS 0
#define KV_AUX " nt"
#define DMA_AUX ""
#define STAMP(i)
#define DI __device__ __forceinline__
#define MFMA32(a, b, c) __builtin_amdgcn_mfma_f32_32x32x16_bf16((a), (b), (c), 0, 0, 0)
#define MFMA16(a, b, c) __builtin_amdgcn_mfma_f32_16x16x32_bf16((a), (b), (c), 0, 0, 0)
#define STR2(x) #x
#define STR(x) STR2(x)
#define WAIT_BAR(N) asm volatile("s_waitcnt vmcnt(" STR(N) ") lgkmcnt(0)\n\ts_barrier" ::: "memory")
#define LDS_BAR() asm volatile("s_waitcnt lgkmcnt(0)\n\ts_barrier" ::: "memory")

constexpr int SQ = 160, SK = 1024, DH = 128, NH = 32, NB = 16, DM = 512, HD = 4096;
constexpr float CSC = 0.088388347648318447f * 1.4426950408889634f;
constexpr float THR_L2 = 8.0f;

DI unsigned cvtpk(float lo, float hi) {
  f32x2 v = {lo, hi};
  bf16x2_t b = __builtin_convertvector(v, bf16x2_t);
  return __builtin_bit_cast(unsigned, b);
}
DI bf16x8 pack8(float a0, float a1, float a2, float a3, float a4, float a5, float a6, float a7) {
  u32x4 w = {cvtpk(a0, a1), cvtpk(a2, a3), cvtpk(a4, a5), cvtpk(a6, a7)};
  return __builtin_bit_cast(bf16x8, w);
}
DI s16x4 trd(lds_cptr p) {
  return __builtin_bit_cast(s16x4, __builtin_amdgcn_ds_read_tr16_b64_v4i16((__attribute__((address_space(3))) v4i16_t*)p));
}
DI bf16x8 tr2(lds_cptr p0, lds_cptr p1) {
  s16x4 a = trd(p0), b = trd(p1);
  return __builtin_shufflevector(a, b, 0, 1, 2, 3, 4, 5, 6, 7);
}
DI bf16x8 ldl(lds_cptr p) { return *(const __attribute__((address_space(3))) bf16x8*)p; }
DI void dma16(const char* sbase, unsigned voff, unsigned lds_dst) {
  asm volatile("s_mov_b32 m0, %2\n\ts_nop 0\n\tglobal_load_lds_dwordx4 %1, %0" DMA_AUX ::"s"(sbase), "v"(voff), "s"(lds_dst) : "memory");
}
DI void dma16s(const char* sbase, unsigned voff, unsigned lds_dst) {
  asm volatile("s_mov_b32 m0, %2\n\ts_nop 0\n\tglobal_load_lds_dwordx4 %1, %0" KV_AUX ::"s"(sbase), "v"(voff), "s"(lds_dst) : "memory");
}
template <int IM, int IMG, bool NT>
DI void bdma(__amdgpu_buffer_rsrc_t rs, unsigned voff, unsigned so, unsigned dst) {
  unsigned tmp;
  if constexpr (NT)
    asm volatile("s_add_u32 m0, %[dst], %[im]\n\ts_add_u32 %[tmp], %[so], %[ig]\n\tbuffer_load_dwordx4 %[v], %[rs], %[tmp] offen nt lds"
                 : [tmp] "=&s"(tmp)
                 : [dst] "s"(dst), [so] "s"(so), [im] "i"(IM), [ig] "i"(IMG), [v] "v"(voff), [rs] "s"(rs)
                 : "memory", "scc");
  else
    asm volatile("s_add_u32 m0, %[dst], %[im]\n\ts_add_u32 %[tmp], %[so], %[ig]\n\tbuffer_load_dwordx4 %[v], %[rs], %[tmp] offen lds"
                 : [tmp] "=&s"(tmp)
                 : [dst] "s"(dst), [so] "s"(so), [im] "i"(IM), [ig] "i"(IMG), [v] "v"(voff), [rs] "s"(rs)
                 : "memory", "scc");
}
DI float swap32max(float x) {
  auto r = __builtin_amdgcn_permlane32_swap(__float_as_uint(x), __float_as_uint(x), false, false);
  return fmaxf(__uint_as_float(r[0]), __uint_as_float(r[1]));
}
DI float swap16max(float x) {
  auto r = __builtin_amdgcn_permlane16_swap(__float_as_uint(x), __float_as_uint(x), false, false);
  return fmaxf(__uint_as_float(r[0]), __uint_as_float(r[1]));
}
DI float swap32sum(float x) {
  auto r = __builtin_amdgcn_permlane32_swap(__float_as_uint(x), __float_as_uint(x), false, false);
  return __uint_as_float(r[0]) + __uint_as_float(r[1]);
}
DI float swap16sum(float x) {
  auto r = __builtin_amdgcn_permlane16_swap(__float_as_uint(x), __float_as_uint(x), false, false);
  return __uint_as_float(r[0]) + __uint_as_float(r[1]);
}

constexpr unsigned SLOT = 16384, XCH = 65536, LDS1 = 81920;
constexpr size_t WS_QPARK = (size_t)56 << 20;
#define SB() __builtin_amdgcn_sched_barrier(0)
#define MX3(a, b, c) __builtin_fmaxf(__builtin_fmaxf((a), (b)), (c))
#define PIN(x) asm volatile("" : "+v"(x))
template <int N, class F, int... I>
DI void static_for_impl(F&& f, std::integer_sequence<int, I...>) { (f(std::integral_constant<int, I>{}), ...); }
template <int N, class F>
DI void static_for(F&& f) { static_for_impl<N>(f, std::make_integer_sequence<int, N>{}); }
DI int lane_id() {
  int l;
  asm volatile("v_mbcnt_lo_u32_b32 %0, -1, 0\n\tv_mbcnt_hi_u32_b32 %0, -1, %0" : "=v"(l));
  return l;
}
DI f32x16 mfma32_head(bf16x8 a, bf16x8 b, const f32x16& c) {
  f32x16 d;
  asm volatile("s_nop 1\n\tv_mfma_f32_32x32x16_bf16 %0, %1, %2, %3" : "=&v"(d) : "v"(a), "v"(b), "v"(c));
  return d;
}
DI f32x4 mfma16_head(bf16x8 a, bf16x8 b, const f32x4& c) {
  f32x4 d;
  asm volatile("s_nop 1\n\tv_mfma_f32_16x16x32_bf16 %0, %1, %2, %3" : "=&v"(d) : "v"(a), "v"(b), "v"(c));
  return d;
}

__global__ __launch_bounds__(256, 2) void attn_kernel(const u16* __restrict__ xA, const u16* __restrict__ kA, const u16* __restrict__ vA,
                                                      const u16* __restrict__ wqA, const u16* __restrict__ xB, const u16* __restrict__ kB,
                                                      const u16* __restrict__ vB, const u16* __restrict__ wqB, u16* __restrict__ o_ws) {
  extern __shared__ __attribute__((aligned(16))) char smem[];
  const int tid = threadIdx.x, lane = tid & 63;
  const int w = __builtin_amdgcn_readfirstlane(tid >> 6);
  const int g = blockIdx.x, xcd = g & 7, idx = g >> 3;
  const int h = 4 * xcd + (idx & 3), b = idx >> 2;
  const unsigned lds0 = (unsigned)(uintptr_t)smem;
  const lds_cptr L = (lds_cptr)smem;
  const lds_ptr LW = (lds_ptr)smem;

  const size_t kvoff = (size_t)(b * NH + h) * SK * DH * 2;
  const char* Kp0 = (const char*)kA + kvoff;
  const char* Vp0 = (const char*)vA + kvoff;
  const char* Kp1 = (const char*)kB + kvoff;
  const char* Vp1 = (const char*)vB + kvoff;
  const char* Wp0 = (const char*)wqA + h * 256;
  const char* Wp1 = (const char*)wqB + h * 256;

  const int r32 = lane & 31, hh = lane >> 5;
  const int r16 = lane & 15, gq = lane >> 4;
  const int q4 = (lane & 15) >> 2, p4 = lane & 3, blk = (lane >> 4) & 1;

  const unsigned rowl = 8 * (w >> 1) + ((lane >> 2) & 7);
  const unsigned chs = 8 * (w & 1) + 4 * (lane >> 5) + ((lane & 3) ^ (2 * (w >> 1) + ((lane >> 4) & 1)));
  const unsigned voffKV = rowl * 256 + chs * 16;

  auto mk_voffE = [&]() {
    const int ln = lane_id();
    return (unsigned)(8 * w + (ln >> 3)) * (DM * 2) + 16 * ((ln & 7) ^ (4 * (w & 1) + (ln >> 4)));
  };
  auto mk_voffX = [&]() {
    const int ln = lane_id();
    return (unsigned)(16 * w + (ln >> 2)) * (DM * 2) + 16 * ((ln & 3) ^ ((ln >> 4) & 3));
  };
  auto mk_voffW = [&]() {
    unsigned v = voffKV;
    asm volatile("" : "+v"(v));
    return ((v & ~255u) << 5) | (v & 255u);
  };
  const char* Xp0 = (const char*)(xA + (size_t)b * SQ * DM);
  const char* Xp1 = (const char*)(xB + (size_t)b * SQ * DM);

  const char *d_src, *d_src2;
  unsigned d_dst;
  bool d_kv;
  int d_nn;
  const bool tb = g >= 256;
  const int w_ = w;
  if (tb) __builtin_amdgcn_s_setprio(1);
  auto prep = [&](int nn) {
    d_dst = lds0 + (unsigned)(nn & 3) * SLOT + (unsigned)w * 1024;
    const int ph = nn >> 4, j = nn & 15;
    d_nn = nn;
    const bool isq = tb ? (ph < 2 || ph >= 6) : (ph == 0 || ph == 3 || ph >= 6);
    const int un = tb ? (ph == 0 || ph == 4 || ph == 5) : (ph >= 3 && ph < 6);
    const int hf = tb ? (ph & 1) : (ph == 2 || ph == 5);
    d_kv = !isq;
    if (isq) {
      d_src = (un ? Wp1 : Wp0) + (size_t)j * (32 * 8192);
      d_src2 = (un ? Xp1 : Xp0) + 64 * j;
    } else {
      d_src = ((j & 1) ? (un ? Vp1 : Vp0) : (un ? Kp1 : Kp0)) + (size_t)(8 * hf + (j >> 1)) * 16384;
      d_src2 = d_src;
    }
  };
  auto piece = [&](int j) {
    const unsigned dst = d_dst + 4096u * (unsigned)j;
    if (d_kv) dma16s(d_src + 4096 * j, voffKV, dst);
    else if (j < 2) dma16(d_src + (size_t)j * (16 * 8192), mk_voffW(), dst);
    else dma16(d_src2 + (size_t)(j - 2) * (64 * 1024), mk_voffX(), dst);
  };
  auto issue = [&](int nn) {
    prep(nn);
    piece(0);
    piece(1);
    piece(2);
    piece(3);
  };
  auto eissue = [&](int u, int e) { dma16((u ? Xp1 : Xp0) + 128 * 1024 + 128 * e, mk_voffE(), lds0 + XCH + 4096u * (unsigned)(e & 3) + (unsigned)w * 1024); };

  unsigned kb0 = lds0 + 2048 * (r32 >> 3) + 64 * (r32 & 7) + 16 * ((0 + hh) ^ ((r32 >> 2) & 3));
  unsigned kb1 = lds0 + 2048 * (r32 >> 3) + 64 * (r32 & 7) + 16 * ((2 + hh) ^ ((r32 >> 2) & 3));
  unsigned vb0 = lds0 + 64 * (4 * hh + q4) + 16 * ((2 * blk + (p4 >> 1)) ^ (0 + hh)) + 8 * (p4 & 1);
  unsigned vb1 = lds0 + 2048 + 64 * (4 * hh + q4) + 16 * ((2 * blk + (p4 >> 1)) ^ (2 + hh)) + 8 * (p4 & 1);
  const int eg = w & 1, kh = w >> 1;
  const int pa = (0x78 >> (2 * (r16 >> 2))) & 3;
  const int pg = (0x78 >> (2 * gq)) & 3;
  unsigned keb = lds0 + 2048 * (4 * kh + (pa >> 1)) + 64 * (4 * (pa & 1) + (r16 & 3)) + 16 * (gq ^ pa);
  unsigned veb0 = lds0 + 2048 * (4 * kh + (pg >> 1)) + 64 * (4 * (pg & 1) + q4) + 16 * ((0 + (p4 >> 1)) ^ pg) + 8 * (p4 & 1);
  unsigned veb1 = lds0 + 2048 * (4 * kh + (pg >> 1)) + 64 * (4 * (pg & 1) + q4) + 16 * ((2 + (p4 >> 1)) ^ pg) + 8 * (p4 & 1);

  if (g == 0 && tid < 128) {
    unsigned* sy = (unsigned*)((char*)o_ws + (size_t)2 * NB * SQ * HD * 2) + tid * 32;
    sy[0] = 0u;
    sy[16] = 0u;
  }
  {
    const int u0 = tb ? 1 : 0;
    eissue(u0, 0);
    eissue(u0, 1);
    eissue(u0, 2);
  }
  issue(0);
  issue(1);
  issue(2);

  bf16x8 qf[8];
  bf16x8 qe[4];
  char* qpark = (char*)o_ws + WS_QPARK + (size_t)(g & 255) * 40960;

  auto qphase = [&](const int u, const int n0, const bool park) __attribute__((always_inline)) {
    STAMP(1);
    {
      const int ln = lane_id();
      const int r32 = ln & 31, hh = ln >> 5, q4 = (ln & 15) >> 2, p4 = ln & 3, blk = (ln >> 4) & 1;
      const int sw = ((p4 & 1) << 1) | (p4 >> 1);
      const unsigned wb0 = 2048 * hh + 64 * (0 + q4) + 16 * ((2 * blk + (sw >> 1)) ^ (2 * hh + 0)) + 8 * (sw & 1);
      const unsigned wb1 = 2048 * hh + 64 * (4 + q4) + 16 * ((2 * blk + (sw >> 1)) ^ (2 * hh + 1)) + 8 * (sw & 1);
      const unsigned xb0 = 8192 + 64 * (32 * w + r32) + 16 * ((0 + hh) ^ ((r32 >> 2) & 3));
      const unsigned xb1 = 8192 + 64 * (32 * w + r32) + 16 * ((2 + hh) ^ ((r32 >> 2) & 3));
      unsigned xe_b[4];
#pragma unroll
      for (int s = 0; s < 4; ++s) xe_b[s] = XCH + 128 * r32 + 16 * ((2 * s + hh) ^ ((r32 >> 1) & 7));
      const unsigned we0 = wb0 + 512 * w, we1 = wb1 + 512 * w;
      f32x16 aq[4] = {};
      f32x16 ae = {};
      const __amdgpu_buffer_rsrc_t rsW = __builtin_amdgcn_make_buffer_rsrc((void*)(u ? Wp1 : Wp0), (short)0, DM * HD * 2, 0x00020000);
      const __amdgpu_buffer_rsrc_t rsX = __builtin_amdgcn_make_buffer_rsrc((void*)(u ? Xp1 : Xp0), (short)0, SQ * DM * 2, 0x00020000);
      const unsigned vW = mk_voffW(), vX = mk_voffX();
      const unsigned qdst = lds0 + (unsigned)w * 1024;
      auto qpiece = [&](auto C_, auto J_) __attribute__((always_inline)) {
        constexpr int cn = decltype(C_)::value, j = decltype(J_)::value;
        if constexpr (cn < 16) {
          if constexpr (j < 2) bdma<(cn & 3) * SLOT + 4096 * j, cn * (32 * 8192) + j * (16 * 8192), false>(rsW, vW, 0u, qdst);
          else bdma<(cn & 3) * SLOT + 4096 * j, 64 * cn + (j - 2) * 65536, false>(rsX, vX, 0u, qdst);
        } else {
          piece(j);
        }
      };
      static_for<16>([&](auto C_) __attribute__((always_inline)) {
        constexpr int c = decltype(C_)::value;
        WAIT_BAR(8);
        const lds_cptr sl = L + (c & 3) * SLOT;
        const lds_cptr sxe = L + 4096 * ((c >> 1) & 3);
        if constexpr (c + 3 >= 16) prep(n0 + c + 3);
        bf16x8 xm[2], xe[2], af[10];
        xm[0] = ldl(sl + xb0);
        af[0] = tr2(sl + wb0, sl + wb1);
        af[1] = tr2(sl + wb0 + 512, sl + wb1 + 512);
        xe[0] = ldl(sxe + xe_b[2 * (c & 1)]);
        SB();
#pragma unroll
        for (int i = 0; i < 10; ++i) {
          const int s = i / 5, dt = i % 5;
          const int i2 = i + 2, s2 = i2 / 5, d2 = i2 % 5;
          if (i2 < 10) {
            if (d2 < 4) af[i2] = tr2(sl + wb0 + 4096 * s2 + 512 * d2, sl + wb1 + 4096 * s2 + 512 * d2);
            else af[i2] = tr2(sl + we0 + 4096 * s2, sl + we1 + 4096 * s2);
          }
          if (i == 2) xm[1] = ldl(sl + xb1);
          if (i == 3) xe[1] = ldl(sxe + xe_b[2 * (c & 1) + 1]);
          if (dt < 4) aq[dt] = MFMA32(af[i], xm[s], aq[dt]);
          else ae = MFMA32(af[i], xe[s], ae);
          if (i == 1) qpiece(std::integral_constant<int, c + 3>{}, std::integral_constant<int, 0>{});
          if (i == 3) qpiece(std::integral_constant<int, c + 3>{}, std::integral_constant<int, 1>{});
          if (i == 5) qpiece(std::integral_constant<int, c + 3>{}, std::integral_constant<int, 2>{});
          if (i == 7) qpiece(std::integral_constant<int, c + 3>{}, std::integral_constant<int, 3>{});
          SB();
        }
        if ((c & 1) == 0) {
          if ((c >> 1) + 3 < 8) eissue(u, (c >> 1) + 3);
          else if (park) eissue(0, (c >> 1) + 3 - 8);
        }
      });
#pragma unroll
      for (int dt = 0; dt < 4; ++dt) {
        aq[dt] *= CSC;
        qf[2 * dt] = pack8(aq[dt][0], aq[dt][1], aq[dt][2], aq[dt][3], aq[dt][4], aq[dt][5], aq[dt][6], aq[dt][7]);
        qf[2 * dt + 1] = pack8(aq[dt][8], aq[dt][9], aq[dt][10], aq[dt][11], aq[dt][12], aq[dt][13], aq[dt][14], aq[dt][15]);
      }
      ae *= CSC;
      bf16x8 e0 = pack8(ae[0], ae[1], ae[2], ae[3], ae[4], ae[5], ae[6], ae[7]);
      bf16x8 e1 = pack8(ae[8], ae[9], ae[10], ae[11], ae[12], ae[13], ae[14], ae[15]);
      if (park) {
#pragma unroll
        for (int i = 0; i < 8; ++i) *(bf16x8*)(qpark + w * 8192 + 1024 * i + 16 * ln) = qf[i];
        *(bf16x8*)(qpark + 32768 + 256 * r32 + 16 * (4 * w + hh)) = e0;
        *(bf16x8*)(qpark + 32768 + 256 * r32 + 16 * (4 * w + 2 + hh)) = e1;
      } else {
        *(__attribute__((address_space(3))) bf16x8*)(LW + XCH + 256 * r32 + 16 * ((4 * w + hh) ^ (r32 & 15))) = e0;
        *(__attribute__((address_space(3))) bf16x8*)(LW + XCH + 256 * r32 + 16 * ((4 * w + 2 + hh) ^ (r32 & 15))) = e1;
        LDS_BAR();
        const int row = 16 * eg + (ln & 15);
#pragma unroll
        for (int s = 0; s < 4; ++s) qe[s] = ldl(L + XCH + 256 * row + 16 * ((4 * s + (ln >> 4)) ^ (row & 15)));
      }
    }
    STAMP(6);
  };

  auto attention = [&](auto U_, const int nk, const bool qe_lds) __attribute__((always_inline)) {
    constexpr int u = decltype(U_)::value;
    f32x16 o[4] = {};
    f32x4 oe[8] = {};
    float l = 0.f, le = 0.f;
    f32x16 negm = {};
    f32x4 negme = {};
    const __amdgpu_buffer_rsrc_t rsK = __builtin_amdgcn_make_buffer_rsrc((void*)(u ? Kp1 : Kp0), (short)0, SK * DH * 2, 0x00020000);
    const __amdgpu_buffer_rsrc_t rsV = __builtin_amdgcn_make_buffer_rsrc((void*)(u ? Vp1 : Vp0), (short)0, SK * DH * 2, 0x00020000);
    auto tile = [&](const int t, auto F_) __attribute__((always_inline)) {
      constexpr bool FAST = decltype(F_)::value;
      const unsigned so = (unsigned)t * 16384u;
      const unsigned fdst = lds0 + (unsigned)w * 1024 + ((unsigned)(t & 1) << 15);
      const unsigned vdst = lds0 + (unsigned)w * 1024 + 3 * SLOT - ((unsigned)(t & 1) << 15);
#define PIECE_V(j) do { if constexpr (FAST) bdma<4096 * (j), 16384 + 4096 * (j), true>(rsV, voffKV, so, vdst); else piece(j); } while (0)
#define PIECE_K(j) do { if constexpr (FAST) bdma<4096 * (j), 32768 + 4096 * (j), true>(rsK, voffKV, so, fdst); else piece(j); } while (0)
      STAMP(7);
      WAIT_BAR(8);
      STAMP(0);
      if constexpr (!FAST) {
        if (u == 0 && t == 14 && !tb) {
          eissue(1, 0);
          eissue(1, 1);
          eissue(1, 2);
        }
        prep(nk + 2 * t + 3);
      }
      const float thr = t == 0 ? -__builtin_inff() : THR_L2;
      const float lo = t == 0 ? -__builtin_inff() : 0.f;
      f32x16 s0, s1;
      f32x4 se0, se1;
      float ma, mb, la = 0.f, lb = 0.f, lf = 0.f;
      u32x4 pw[4];
      u32x4 pwe;
#define EX(x) __builtin_amdgcn_exp2f(x)
#define P2(S, i, DST) do { const float a_ = EX(S[i]), b_ = EX(S[(i) + 1]); la += a_; lb += b_; unsigned d_ = cvtpk(a_, b_); PIN(d_); PIN(la); PIN(lb); DST = d_; } while (0)
#define P2E(S, i, DST) do { const float a_ = EX(S[i]), b_ = EX(S[(i) + 1]); le += a_; lf += b_; unsigned d_ = cvtpk(a_, b_); PIN(d_); PIN(le); PIN(lf); DST = d_; } while (0)
      const lds_cptr k0 = (lds_cptr)(uintptr_t)kb0, k1 = (lds_cptr)(uintptr_t)kb1, ke = (lds_cptr)(uintptr_t)keb;
      bf16x8 kf[16], ef[8];
#define KRD(i) kf[i] = ldl((((i) & 1) ? k1 : k0) + 512 * (((i) & 7) >> 1) + 8192 * ((i) >> 3))
#define ERD(i) ef[i] = ldl(ke + 512 * ((i) >> 1) + 4096 * ((i) & 1))
      KRD(0); KRD(1); KRD(2); KRD(3);
      SB();
#pragma unroll
      for (int i = 0; i < 8; ++i) {
        if (i + 4 < 8) { KRD(i + 4); } else { ERD(i - 4); }
        if (i == 0) s0 = mfma32_head(kf[i], qf[i], negm);
        else s0 = MFMA32(kf[i], qf[i], s0);
        if (i == 2) PIECE_V(0);
        if (i == 6) PIECE_V(1);
        SB();
      }
#pragma unroll
      for (int i = 0; i < 8; ++i) {
        if (i + 4 < 8) { ERD(i + 4); } else { KRD(i + 4); }
        if (i == 0) se0 = mfma16_head(ef[i], qe[0], negme);
        else if (i == 1) se1 = mfma16_head(ef[i], qe[0], negme);
        else if (i & 1) se1 = MFMA16(ef[i], qe[i >> 1], se1);
        else se0 = MFMA16(ef[i], qe[i >> 1], se0);
        if (i == 3) { ma = MX3(s0[0], s0[1], s0[2]); mb = MX3(s0[3], s0[4], s0[5]); PIN(ma); PIN(mb); }
        if (i == 4) { ma = MX3(ma, s0[6], s0[7]); mb = MX3(mb, s0[8], s0[9]); PIN(ma); PIN(mb); }
        if (i == 5) { ma = MX3(ma, s0[10], s0[11]); mb = MX3(mb, s0[12], s0[13]); PIN(ma); PIN(mb); }
        if (i == 6) { ma = MX3(ma, s0[14], s0[15]); ma = swap32max(__builtin_fmaxf(ma, mb)); PIN(ma); }
        SB();
      }
      {
        const float pm = ma;
        if (__builtin_expect(__any(pm > thr), 0)) {
          const float d = fmaxf(pm, lo);
          const float al = __builtin_amdgcn_exp2f(-fmaxf(d, 0.f));
          l *= al;
#pragma unroll
          for (int c = 0; c < 4; ++c) o[c] *= al;
          s0 -= d;
          negm -= d;
        }
      }
      SB();
      float pe;
#pragma unroll
      for (int i = 0; i < 8; ++i) {
        if (i + 4 < 8) { KRD(i + 12); }
        if (i == 0) s1 = mfma32_head(kf[8 + i], qf[i], negm);
        else s1 = MFMA32(kf[8 + i], qf[i], s1);
        P2(s0, 2 * i, pw[i >> 2][i & 3]);
        if (i == 1) PIECE_V(2);
        if (i == 5) PIECE_V(3);
        if (i == 2) { pe = MX3(se0[0], se0[1], se0[2]); pe = MX3(pe, se0[3], se1[0]); PIN(pe); }
        if (i == 3) { pe = MX3(pe, se1[1], se1[2]); pe = __builtin_fmaxf(pe, se1[3]); PIN(pe); }
        if (i == 4) { pe = swap16max(pe); PIN(pe); }
        if (i == 6) { pe = swap32max(pe); PIN(pe); }
        SB();
      }
#undef KRD
#undef ERD
      {
        ma = MX3(s1[0], s1[1], s1[2]); mb = MX3(s1[3], s1[4], s1[5]);
        ma = MX3(ma, s1[6], s1[7]); mb = MX3(mb, s1[8], s1[9]);
        ma = MX3(ma, s1[10], s1[11]); mb = MX3(mb, s1[12], s1[13]);
        ma = MX3(ma, s1[14], s1[15]);
        const float pm = swap32max(__builtin_fmaxf(ma, mb));
        if (__builtin_expect(__any((pm > THR_L2) | (pe > thr)), 0)) {
          const float d = fmaxf(pm, 0.f);
          const float al = __builtin_amdgcn_exp2f(-d);
          l *= al;
          la *= al;
          lb *= al;
#pragma unroll
          for (int c = 0; c < 4; ++c) o[c] *= al;
#pragma unroll
          for (int i = 0; i < 8; ++i) {
            const unsigned v = pw[i >> 2][i & 3];
            pw[i >> 2][i & 3] = cvtpk(__uint_as_float(v << 16) * al, __uint_as_float(v & 0xffff0000u) * al);
          }
          s1 -= d;
          negm -= d;
          const float de = fmaxf(pe, lo);
          const float ale = __builtin_amdgcn_exp2f(-fmaxf(de, 0.f));
          le *= ale;
#pragma unroll
          for (int c = 0; c < 8; ++c) oe[c] *= ale;
          se0 -= de;
          se1 -= de;
          negme -= de;
        }
      }
      STAMP(2);
      WAIT_BAR(8);
      STAMP(3);
      if constexpr (!FAST) prep(nk + 2 * t + 4);
      {
        const lds_cptr v0 = (lds_cptr)(uintptr_t)vb0 + SLOT, v1 = (lds_cptr)(uintptr_t)vb1 + SLOT;
        const lds_cptr e0 = (lds_cptr)(uintptr_t)veb0 + SLOT, e1 = (lds_cptr)(uintptr_t)veb1 + SLOT;
        bf16x8 vf[16], wf[8];
#define VRD(i) vf[i] = tr2(v0 + 4096 * ((i) >> 2) + 512 * ((i) & 3), v1 + 4096 * ((i) >> 2) + 512 * ((i) & 3))
#define WRD(i) wf[i] = tr2((((i) & 1) ? e1 : e0) + 512 * ((i) >> 1), (((i) & 1) ? e1 : e0) + 512 * ((i) >> 1) + 4096)
        VRD(0); VRD(1); VRD(2);
        SB();
#pragma unroll
        for (int i = 0; i < 16; ++i) {
          const int s = i >> 2, c = i & 3;
          if (i + 3 < 16) { VRD(i + 3); } else { WRD(i - 13); }
          o[c] = MFMA32(vf[i], __builtin_bit_cast(bf16x8, pw[s]), o[c]);
          if (i < 8) P2(s1, 2 * i, pw[2 + (i >> 2)][i & 3]);
          if (i >= 8 && (i & 1) == 0) { const int j = (i - 8) >> 1; if (j < 2) P2E(se0, 2 * j, pwe[j]); else P2E(se1, 2 * j - 4, pwe[j]); }
          if (i == 1) PIECE_K(0);
          if (i == 5) PIECE_K(1);
          if (i == 9) PIECE_K(2);
          if (i == 13) PIECE_K(3);
          SB();
        }
#pragma unroll
        for (int i = 0; i < 8; ++i) {
          if (i + 3 < 8) { WRD(i + 3); }
          oe[i] = MFMA16(wf[i], __builtin_bit_cast(bf16x8, pwe), oe[i]);
          SB();
        }
#undef VRD
#undef WRD
      }
      l += la + lb;
      le += lf;
#undef EX
#undef P2
#undef P2E
      STAMP(5);
      const unsigned dir = (t & 1) ? 0u - 2 * SLOT : 2 * SLOT;
      kb0 += dir; kb1 += dir; keb += dir; vb0 += dir; vb1 += dir; veb0 += dir; veb1 += dir;
      PIN(kb0); PIN(kb1); PIN(keb); PIN(vb0); PIN(vb1); PIN(veb0); PIN(veb1);
#undef PIECE_V
#undef PIECE_K
    };
#pragma unroll 1
    for (int t = 0; t < 14; ++t) tile(t, std::true_type{});
#pragma unroll 1
    for (int t = 14; t < 16; ++t) tile(t, std::false_type{});

    char* ob = (char*)o_ws + (size_t)((u * NB + b) * NH + h) * (40 * 1024);
    const int ln = lane_id();
    int w = w_;
    asm volatile("" : "+s"(w));
    const int eg = w & 1, kh = w >> 1;
    {
      l = swap32sum(l);
      const float inv = 1.0f / l;
      char* of = ob + w * 8192 + 16 * ln;
#pragma unroll
      for (int c = 0; c < 4; ++c) {
        bf16x8 v0 = pack8(o[c][0] * inv, o[c][1] * inv, o[c][2] * inv, o[c][3] * inv, o[c][4] * inv, o[c][5] * inv, o[c][6] * inv, o[c][7] * inv);
        bf16x8 v1 = pack8(o[c][8] * inv, o[c][9] * inv, o[c][10] * inv, o[c][11] * inv, o[c][12] * inv, o[c][13] * inv, o[c][14] * inv, o[c][15] * inv);
        *(bf16x8*)(of + 2048 * c) = v0;
        *(bf16x8*)(of + 2048 * c + 1024) = v1;
      }
    }
    {
      constexpr unsigned EX0 = 3 * SLOT;
      const int lane = ln, r16 = ln & 15, gq = ln >> 4;
      const float me = -negme[0];
      le = swap16sum(le);
      le = swap32sum(le);
      LDS_BAR();
      if (lane < 16) {
        f32x2 ml = {me, le};
        *(__attribute__((address_space(3))) f32x2*)(LW + EX0 + 4096 * (w ^ 2) + 8 * lane) = ml;
      }
      LDS_BAR();
      f32x2 pml = *(const __attribute__((address_space(3))) f32x2*)(L + EX0 + 4096 * w + 8 * r16);
      const float M = fmaxf(me, pml[0]);
      const float sc = __builtin_amdgcn_exp2f(me - M), scp = __builtin_amdgcn_exp2f(pml[0] - M);
      const float f = sc / (le * sc + pml[1] * scp);
      asm volatile("s_waitcnt lgkmcnt(0)" ::: "memory");
      f32x4 keep[4];
#pragma unroll
      for (int i = 0; i < 4; ++i) {
        f32x4 snd;
#pragma unroll
        for (int r = 0; r < 4; ++r) {
          snd[r] = (kh ? oe[i][r] : oe[4 + i][r]) * f;
          keep[i][r] = (kh ? oe[4 + i][r] : oe[i][r]) * f;
        }
        *(__attribute__((address_space(3))) f32x4*)(LW + EX0 + 4096 * w + 1024 * i + 16 * lane) = snd;
      }
      LDS_BAR();
      char* ef = ob + 4 * 8192 + 4096 * kh + 16 * (32 * (gq & 1) + 16 * eg + r16) + 8 * (gq >> 1);
#pragma unroll
      for (int i = 0; i < 4; ++i) {
        f32x4 rcv = *(const __attribute__((address_space(3))) f32x4*)(L + EX0 + 4096 * (w ^ 2) + 1024 * i + 16 * lane);
        u32x2 v = {cvtpk(keep[i][0] + rcv[0], keep[i][1] + rcv[1]), cvtpk(keep[i][2] + rcv[2], keep[i][3] + rcv[3])};
        *(u32x2*)(ef + 1024 * i) = v;
      }
    }
  };
#pragma unroll 1
  for (int i = 0; i < (tb ? 2 : 1); ++i) qphase((tb && i == 0) ? 1 : 0, 16 * i, tb && i == 0);
  attention(std::integral_constant<int, 0>{}, tb ? 32 : 16, true);
  __builtin_amdgcn_s_setprio(0);
  if (tb) {
    const int ln = lane_id();
#pragma unroll
    for (int i = 0; i < 8; ++i) qf[i] = *(const bf16x8*)(qpark + w * 8192 + 1024 * i + 16 * ln);
#pragma unroll
    for (int s = 0; s < 4; ++s) qe[s] = *(const bf16x8*)(qpark + 32768 + 256 * (16 * eg + (ln & 15)) + 16 * (4 * s + (ln >> 4)));
#pragma unroll
    for (int i = 0; i < 8; ++i) PIN(qf[i]);
#pragma unroll
    for (int s = 0; s < 4; ++s) PIN(qe[s]);
  } else {
    qphase(1, 48, false);
  }
  attention(std::integral_constant<int, 1>{}, 64, !tb);
  asm volatile("s_waitcnt vmcnt(0)" ::: "memory");
}

constexpr unsigned S2 = 36864, NS2 = 4, LDS2 = S2 * NS2;
constexpr size_t WS_SYNC = (size_t)2 * NB * SQ * HD * 2;
constexpr size_t WS_PART = WS_SYNC + 128 * 128;

__global__ __launch_bounds__(512, 2) void oproj_kernel(char* __restrict__ ws, const u16* __restrict__ woA, const u16* __restrict__ woB,
                                                       u16* __restrict__ out) {
  extern __shared__ __attribute__((aligned(16))) char smem[];
  const u16* o_ws = (const u16*)ws;
  const int tid = threadIdx.x, lane = tid & 63;
  const int w = __builtin_amdgcn_readfirstlane(tid >> 6);
  const int wn = w & 3, wk = w >> 2;
  const int g = blockIdx.x, xcd = g & 7, idx = g >> 3;
  const int blkk = xcd >> 2, mt = 4 * (xcd & 3) + (idx >> 3), nt = (idx >> 1) & 3, kh = idx & 1;
  const int tile = (blkk * 16 + mt) * 4 + nt;
  const unsigned lds0 = (unsigned)(uintptr_t)smem;
  const lds_cptr L = (lds_cptr)smem;
  const lds_ptr LW = (lds_ptr)smem;
  const int r32 = lane & 31, hh = lane >> 5;
  const int q4 = (lane & 15) >> 2, p4 = lane & 3, blk = (lane >> 4) & 1;
  const char* src0;
  unsigned voff, dst0;
  if (wk == 0) {
    src0 = (const char*)o_ws + (size_t)((blkk * NB + mt) * NH + kh * 16) * (40 * 1024) + 1024 * wn;
    voff = 16 * lane;
    dst0 = lds0 + (unsigned)wn * 1024;
  } else {
    src0 = (const char*)((blkk ? woB : woA) + (size_t)(kh * 2048) * DM + nt * 128);
    voff = (unsigned)(8 * (wn >> 1) + ((lane >> 2) & 7)) * (DM * 2) + 128 * (wn & 1) + 64 * (lane >> 5) + 16 * (lane & 3);
    dst0 = lds0 + 20480 + (unsigned)wn * 1024;
  }
  const __amdgpu_buffer_rsrc_t rsd = __builtin_amdgcn_make_buffer_rsrc((void*)src0, (short)0, 4 << 20, 0x00020000);
  unsigned c_so, c_dst;
  auto cprep = [&](int c) {
    const unsigned cc = (unsigned)c & 31u;
    c_so = wk == 0 ? (cc >> 1) * 40960u + (cc & 1) * 4096u : cc * (64u * DM * 2);
    c_dst = dst0 + (unsigned)(c & 3) * S2;
  };
#define PIECE2(j) do { if (wk == 0) bdma<4096 * (j), 8192 * (j), false>(rsd, voff, c_so, c_dst); else if ((j) < 4) bdma<4096 * (j), 16 * DM * 2 * (j), false>(rsd, voff, c_so, c_dst); } while (0)
  const unsigned ob0 = 2048 * wk + 16 * lane;
  const unsigned wb = 20480 + 8192 * wk + 512 * wn + 64 * (4 * hh + q4) + 32 * blk + 8 * p4;
#define DUMP()
  f32x16 acc[5] = {};
  bf16x8 fa[2][2], fb[2][2][5];
  auto rdA = [&](int buf, const lds_cptr sl, int s2) { fa[buf][s2] = tr2(sl + wb + 4096 * s2, sl + wb + 4096 * s2 + 2048); };
  auto rdB = [&](int buf, const lds_cptr sl, int s2, int i) { fb[buf][s2][i] = ldl(sl + ob0 + 1024 * s2 + 4096 * i); };
#pragma unroll
  for (int c = 0; c < 4; ++c) {
    cprep(c);
    PIECE2(0); PIECE2(1); PIECE2(2); PIECE2(3); PIECE2(4);
  }
  if (wk == 0) WAIT_BAR(15);
  else WAIT_BAR(12);
#pragma unroll
  for (int s2 = 0; s2 < 2; ++s2) {
    rdA(0, L, s2);
#pragma unroll
    for (int i = 0; i < 5; ++i) rdB(0, L, s2, i);
  }
  auto step = [&](int c, auto B_) __attribute__((always_inline)) {
    constexpr int bf = decltype(B_)::value;
    if (wk == 0) WAIT_BAR(10);
    else WAIT_BAR(8);
    const lds_cptr sl = L + ((c + 1) & 3) * S2;
    cprep(c + 4);
    SB();
#pragma unroll
    for (int s2 = 0; s2 < 2; ++s2) {
#pragma unroll
      for (int i = 0; i < 5; ++i) {
        const int k = 5 * s2 + i;
        if (k == 0) { rdA(bf ^ 1, sl, 0); rdB(bf ^ 1, sl, 0, 0); }
        if (k == 1) { rdB(bf ^ 1, sl, 0, 1); rdB(bf ^ 1, sl, 0, 2); }
        if (k == 2) { rdB(bf ^ 1, sl, 0, 3); rdB(bf ^ 1, sl, 0, 4); }
        if (k == 3) { rdA(bf ^ 1, sl, 1); rdB(bf ^ 1, sl, 1, 0); }
        if (k == 4) { rdB(bf ^ 1, sl, 1, 1); rdB(bf ^ 1, sl, 1, 2); }
        if (k == 5) { rdB(bf ^ 1, sl, 1, 3); rdB(bf ^ 1, sl, 1, 4); }
        acc[i] = MFMA32(fa[bf][s2], fb[bf][s2][i], acc[i]);
        if (k == 1) PIECE2(0);
        if (k == 3) PIECE2(1);
        if (k == 5) PIECE2(2);
        if (k == 7) PIECE2(3);
        if (k == 9) PIECE2(4);
        SB();
      }
    }
  };
  STAMP(0);
#pragma unroll 1
  for (int c = 0; c < 32; c += 2) {
    step(c, std::integral_constant<int, 0>{});
    step(c + 1, std::integral_constant<int, 1>{});
  }
  STAMP(1);
  WAIT_BAR(0);
  f32x4 res[5][2];
  auto red = [&](auto K_) __attribute__((always_inline)) {
    constexpr int kk = decltype(K_)::value;
#pragma unroll
    for (int i = 0; i < 5; ++i)
#pragma unroll
      for (int z = 0; z < 2; ++z) {
        f32x4 v;
#pragma unroll
        for (int r = 0; r < 4; ++r) v[r] = acc[i][8 * (1 - kk) + 4 * z + r];
        *(__attribute__((address_space(3))) f32x4*)(LW + 10240 * w + 2048 * i + 1024 * z + 16 * lane) = v;
      }
    LDS_BAR();
#pragma unroll
    for (int i = 0; i < 5; ++i)
#pragma unroll
      for (int z = 0; z < 2; ++z) {
        f32x4 v = *(const __attribute__((address_space(3))) f32x4*)(L + 10240 * (w ^ 4) + 2048 * i + 1024 * z + 16 * lane);
#pragma unroll
        for (int r = 0; r < 4; ++r) res[i][z][r] = acc[i][8 * kk + 4 * z + r] + v[r];
      }
  };
  if (wk) red(std::integral_constant<int, 1>{});
  else red(std::integral_constant<int, 0>{});
  STAMP(2);
  unsigned* sync = (unsigned*)(ws + WS_SYNC + (size_t)tile * 128);
  volatile __attribute__((address_space(3))) unsigned* tk = (volatile __attribute__((address_space(3))) unsigned*)(LW + 90112);
  if (tid == 0) *tk = __hip_atomic_fetch_add(sync, 1u, __ATOMIC_RELAXED, __HIP_MEMORY_SCOPE_AGENT);
  LDS_BAR();
  const unsigned ticket = __builtin_amdgcn_readfirstlane(*tk);
  const __amdgpu_buffer_rsrc_t rs = __builtin_amdgcn_make_buffer_rsrc(ws + WS_PART + (size_t)tile * 81920, 0, 81920, 0x00020000);
  const unsigned poff = (unsigned)w * 10240 + 16 * lane;
  if (ticket == 0) {
#pragma unroll
    for (int i = 0; i < 5; ++i)
#pragma unroll
      for (int z = 0; z < 2; ++z)
        __builtin_amdgcn_raw_buffer_store_b128(__builtin_bit_cast(u32x4, res[i][z]), rs, poff + 2048 * i + 1024 * z, 0, 16);
    asm volatile("s_waitcnt vmcnt(0)" ::: "memory");
    __builtin_amdgcn_s_barrier();
    if (tid == 0) __hip_atomic_store(sync + 16, 1u, __ATOMIC_RELAXED, __HIP_MEMORY_SCOPE_AGENT);
    STAMP(3);
    DUMP();
    return;
  }
  if (tid == 0) {
    while (__hip_atomic_load(sync + 16, __ATOMIC_RELAXED, __HIP_MEMORY_SCOPE_AGENT) == 0u) __builtin_amdgcn_s_sleep(1);
    __builtin_amdgcn_fence(__ATOMIC_ACQUIRE, "agent");
    asm volatile("s_waitcnt vmcnt(0)" ::: "memory");
  }
  __syncthreads();
  STAMP(4);
#pragma unroll
  for (int i = 0; i < 5; ++i) {
    u16* orow = out + (size_t)(mt * 160 + 32 * i + r32) * 1024 + blkk * 512 + nt * 128 + 32 * wn + 16 * wk + 4 * hh;
#pragma unroll
    for (int z = 0; z < 2; ++z) {
      const f32x4 v = __builtin_bit_cast(f32x4, __builtin_amdgcn_raw_buffer_load_b128(rs, poff + 2048 * i + 1024 * z, 0, 16));
      u32x2 pv = {cvtpk(res[i][z][0] + v[0], res[i][z][1] + v[1]), cvtpk(res[i][z][2] + v[2], res[i][z][3] + v[3])};
      *(u32x2*)(orow + 8 * z) = pv;
    }
  }
  STAMP(5);
  DUMP();
}

extern "C" void kernel_launch(void* const* d_in, const int* in_sizes, int n_in, void* d_out, int out_size, void* d_ws, size_t ws_size,
                              hipStream_t stream) {
  const u16* xA = (const u16*)d_in[0];
  const u16* kA = (const u16*)d_in[1];
  const u16* vA = (const u16*)d_in[2];
  const u16* wqA = (const u16*)d_in[3];
  const u16* woA = (const u16*)d_in[4];
  const u16* xB = (const u16*)d_in[5];
  const u16* kB = (const u16*)d_in[6];
  const u16* vB = (const u16*)d_in[7];
  const u16* wqB = (const u16*)d_in[8];
  const u16* woB = (const u16*)d_in[9];
  u16* o_ws = (u16*)d_ws;
  (void)hipFuncSetAttribute((const void*)attn_kernel, hipFuncAttributeMaxDynamicSharedMemorySize, (int)LDS1);
  (void)hipFuncSetAttribute((const void*)oproj_kernel, hipFuncAttributeMaxDynamicSharedMemorySize, (int)LDS2);
  hipLaunchKernelGGL(attn_kernel, dim3(512), dim3(256), LDS1, stream, xA, kA, vA, wqA, xB, kB, vB, wqB, o_ws);
  hipLaunchKernelGGL(oproj_kernel, dim3(256), dim3(512), LDS2, stream, (char*)d_ws, woA, woB, (u16*)d_out);
}
```

```cpp
#include <hip/hip_runtime.h>
#include <hip/hip_bf16.h>
#include <stdint.h>
#include <type_traits>
#include <utility>

typedef short bf16x8 __attribute__((ext_vector_type(8)));
typedef short s16x4 __attribute__((ext_vector_type(4)));
typedef float f32x4 __attribute__((ext_vector_type(4)));
typedef float f32x2 __attribute__((ext_vector_type(2)));
typedef __bf16 bf16x2_t __attribute__((ext_vector_type(2)));
typedef unsigned u32x4 __attribute__((ext_vector_type(4)));
typedef unsigned u32x2 __attribute__((ext_vector_type(2)));
typedef unsigned short u16;
typedef __attribute__((address_space(3))) char* lds_ptr;
typedef __attribute__((address_space(3))) const char* lds_cptr;
typedef short v4i16_t __attribute__((ext_vector_type(4)));

#define STAMP(i)
#define DI __device__ __forceinline__
#define MFMA16(a, b, c) __builtin_amdgcn_mfma_f32_16x16x32_bf16((a), (b), (c), 0, 0, 0)
#define STR2(x) #x
#define STR(x) STR2(x)
#define WAIT_BAR(N) asm volatile("s_waitcnt vmcnt(" STR(N) ") lgkmcnt(0)\n\ts_barrier" ::: "memory")
#define LDS_BAR() asm volatile("s_waitcnt lgkmcnt(0)\n\ts_barrier" ::: "memory")
#define SB() __builtin_amdgcn_sched_barrier(0)
#define MX3(a, b, c) __builtin_fmaxf(__builtin_fmaxf((a), (b)), (c))
#define PIN(x) asm volatile("" : "+v"(x))

constexpr int SQ = 160, SK = 1024, DH = 128, NH = 32, NB = 16, DM = 512, HD = 4096;
constexpr float CSC = 0.088388347648318447f * 1.4426950408889634f;
constexpr float THR_L2 = 8.0f;

template <int N, class F, int... I>
DI void static_for_impl(F&& f, std::integer_sequence<int, I...>) { (f(std::integral_constant<int, I>{}), ...); }
template <int N, class F>
DI void static_for(F&& f) { static_for_impl<N>(f, std::make_integer_sequence<int, N>{}); }

DI unsigned cvtpk(float lo, float hi) {
  f32x2 v = {lo, hi};
  bf16x2_t b = __builtin_convertvector(v, bf16x2_t);
  return __builtin_bit_cast(unsigned, b);
}
DI bf16x8 pack44(const f32x4& a, const f32x4& b) {
  u32x4 w = {cvtpk(a[0], a[1]), cvtpk(a[2], a[3]), cvtpk(b[0], b[1]), cvtpk(b[2], b[3])};
  return __builtin_bit_cast(bf16x8, w);
}
DI s16x4 trd(lds_cptr p) {
  return __builtin_bit_cast(s16x4, __builtin_amdgcn_ds_read_tr16_b64_v4i16((__attribute__((address_space(3))) v4i16_t*)p));
}
DI bf16x8 tr2(lds_cptr p0, lds_cptr p1) {
  s16x4 a = trd(p0), b = trd(p1);
  return __builtin_shufflevector(a, b, 0, 1, 2, 3, 4, 5, 6, 7);
}
DI bf16x8 ldl(lds_cptr p) { return *(const __attribute__((address_space(3))) bf16x8*)p; }
DI int lane_id() {
  int l;
  asm volatile("v_mbcnt_lo_u32_b32 %0, -1, 0\n\tv_mbcnt_hi_u32_b32 %0, -1, %0" : "=v"(l));
  return l;
}
DI void dma16(const char* sbase, unsigned voff, unsigned lds_dst) {
  asm volatile("s_mov_b32 m0, %2\n\ts_nop 0\n\tglobal_load_lds_dwordx4 %1, %0" ::"s"(sbase), "v"(voff), "s"(lds_dst) : "memory");
}
DI void dma16s(const char* sbase, unsigned voff, unsigned lds_dst) {
  asm volatile("s_mov_b32 m0, %2\n\ts_nop 0\n\tglobal_load_lds_dwordx4 %1, %0 nt" ::"s"(sbase), "v"(voff), "s"(lds_dst) : "memory");
}
template <int IM, int IMG, bool NT>
DI void bdma(__amdgpu_buffer_rsrc_t rs, unsigned voff, unsigned so, unsigned dst) {
  unsigned tmp;
  if constexpr (NT)
    asm volatile("s_add_u32 m0, %[dst], %[im]\n\ts_add_u32 %[tmp], %[so], %[ig]\n\tbuffer_load_dwordx4 %[v], %[rs], %[tmp] offen nt lds"
                 : [tmp] "=&s"(tmp)
                 : [dst] "s"(dst), [so] "s"(so), [im] "i"(IM), [ig] "i"(IMG), [v] "v"(voff), [rs] "s"(rs)
                 : "memory", "scc");
  else
    asm volatile("s_add_u32 m0, %[dst], %[im]\n\ts_add_u32 %[tmp], %[so], %[ig]\n\tbuffer_load_dwordx4 %[v], %[rs], %[tmp] offen lds"
                 : [tmp] "=&s"(tmp)
                 : [dst] "s"(dst), [so] "s"(so), [im] "i"(IM), [ig] "i"(IMG), [v] "v"(voff), [rs] "s"(rs)
                 : "memory", "scc");
}
DI float rowmax4(float x) {
  auto r = __builtin_amdgcn_permlane16_swap(__float_as_uint(x), __float_as_uint(x), false, false);
  x = fmaxf(__uint_as_float(r[0]), __uint_as_float(r[1]));
  auto s = __builtin_amdgcn_permlane32_swap(__float_as_uint(x), __float_as_uint(x), false, false);
  return fmaxf(__uint_as_float(s[0]), __uint_as_float(s[1]));
}
DI float rowsum4(float x) {
  auto r = __builtin_amdgcn_permlane16_swap(__float_as_uint(x), __float_as_uint(x), false, false);
  x = __uint_as_float(r[0]) + __uint_as_float(r[1]);
  auto s = __builtin_amdgcn_permlane32_swap(__float_as_uint(x), __float_as_uint(x), false, false);
  return __uint_as_float(s[0]) + __uint_as_float(s[1]);
}
DI f32x4 mfma16_head(bf16x8 a, bf16x8 b, const f32x4& c) {
  f32x4 d;
  asm volatile("v_mfma_f32_16x16x32_bf16 %0, %1, %2, %3" : "=&v"(d) : "v"(a), "v"(b), "v"(c));
  return d;
}

constexpr unsigned SLOT = 16384, XCH = 65536, LDS1 = 81920;
constexpr size_t WS_SYNC = (size_t)2 * NB * SQ * HD * 2;
constexpr size_t WS_PART = WS_SYNC + 128 * 128;
constexpr size_t WS_QPARK = (size_t)56 << 20;

__global__ __launch_bounds__(256, 2) void attn_kernel(const u16* __restrict__ xA, const u16* __restrict__ kA, const u16* __restrict__ vA,
                                                      const u16* __restrict__ wqA, const u16* __restrict__ xB, const u16* __restrict__ kB,
                                                      const u16* __restrict__ vB, const u16* __restrict__ wqB, u16* __restrict__ o_ws) {
  extern __shared__ __attribute__((aligned(16))) char smem[];
  const int tid = threadIdx.x, lane = tid & 63;
  const int w = __builtin_amdgcn_readfirstlane(tid >> 6);
  const int g = blockIdx.x, xcd = g & 7, idx = g >> 3;
  const int h = 4 * xcd + (idx & 3), b = idx >> 2;
  const unsigned lds0 = (unsigned)(uintptr_t)smem;
  const lds_cptr L = (lds_cptr)smem;
  const lds_ptr LW = (lds_ptr)smem;

  const size_t kvoff = (size_t)(b * NH + h) * SK * DH * 2;
  const char* Kp0 = (const char*)kA + kvoff;
  const char* Vp0 = (const char*)vA + kvoff;
  const char* Kp1 = (const char*)kB + kvoff;
  const char* Vp1 = (const char*)vB + kvoff;
  const char* Wp0 = (const char*)wqA + h * 256;
  const char* Wp1 = (const char*)wqB + h * 256;
  const char* Xp0 = (const char*)(xA + (size_t)b * SQ * DM);
  const char* Xp1 = (const char*)(xB + (size_t)b * SQ * DM);

  const unsigned rowl = 8 * (w >> 1) + ((lane >> 2) & 7);
  const unsigned chs = 8 * (w & 1) + 4 * (lane >> 5) + ((lane & 3) ^ (2 * (w >> 1) + ((lane >> 4) & 1)));
  const unsigned voffKV = rowl * 256 + chs * 16;
  auto mk_voffE = [&]() {
    const int ln = lane_id();
    return (unsigned)(8 * w + (ln >> 3)) * (DM * 2) + 16 * ((ln & 7) ^ (4 * (w & 1) + (ln >> 4)));
  };
  auto mk_voffX = [&]() {
    const int ln = lane_id();
    return (unsigned)(16 * w + (ln >> 2)) * (DM * 2) + 16 * ((ln & 3) ^ ((ln >> 4) & 3));
  };
  auto mk_voffW = [&]() {
    unsigned v = voffKV;
    asm volatile("" : "+v"(v));
    return ((v & ~255u) << 5) | (v & 255u);
  };

  const bool tb = g >= 256;
  const int w_ = w;
  if (tb) __builtin_amdgcn_s_setprio(1);

  const char *d_src, *d_src2;
  unsigned d_dst;
  bool d_kv;
  auto prep = [&](int nn) {
    d_dst = lds0 + (unsigned)(nn & 3) * SLOT + (unsigned)w * 1024;
    const int ph = nn >> 4, j = nn & 15;
    const bool isq = tb ? (ph < 2 || ph >= 6) : (ph == 0 || ph == 3 || ph >= 6);
    const int un = tb ? (ph == 0 || ph == 4 || ph == 5) : (ph >= 3 && ph < 6);
    const int hf = tb ? (ph & 1) : (ph == 2 || ph == 5);
    d_kv = !isq;
    if (isq) {
      d_src = (un ? Wp1 : Wp0) + (size_t)j * (32 * 8192);
      d_src2 = (un ? Xp1 : Xp0) + 64 * j;
    } else {
      d_src = ((j & 1) ? (un ? Vp1 : Vp0) : (un ? Kp1 : Kp0)) + (size_t)(8 * hf + (j >> 1)) * 16384;
      d_src2 = d_src;
    }
  };
  auto piece = [&](int j) {
    const unsigned dst = d_dst + 4096u * (unsigned)j;
    if (d_kv) dma16s(d_src + 4096 * j, voffKV, dst);
    else if (j < 2) dma16(d_src + (size_t)j * (16 * 8192), mk_voffW(), dst);
    else dma16(d_src2 + (size_t)(j - 2) * (64 * 1024), mk_voffX(), dst);
  };
  auto issue = [&](int nn) {
    prep(nn);
    piece(0);
    piece(1);
    piece(2);
    piece(3);
  };
  auto eissue = [&](int u, int e) { dma16((u ? Xp1 : Xp0) + 128 * 1024 + 128 * e, mk_voffE(), lds0 + XCH + 4096u * (unsigned)(e & 3) + (unsigned)w * 1024); };

  const int eg = w & 1, kh = w >> 1;
  unsigned kA_, kB_, vA0, vA1, vB0, vB1;
  {
    const int r16 = lane & 15, gq = lane >> 4, q4 = (lane & 15) >> 2, p4 = lane & 3;
    const int pa = (0x78 >> (2 * (r16 >> 2))) & 3, pg = (0x78 >> (2 * gq)) & 3;
    const unsigned kb = lds0 + 2048 * (pa >> 1) + 64 * (4 * (pa & 1) + (r16 & 3)) + 16 * (gq ^ pa);
    kA_ = kb + 8192 * kh;
    kB_ = kb + 8192 * (1 - kh);
    const unsigned v0 = lds0 + SLOT + 2048 * (pg >> 1) + 64 * (4 * (pg & 1) + q4) + 16 * ((0 + (p4 >> 1)) ^ pg) + 8 * (p4 & 1);
    const unsigned v1 = lds0 + SLOT + 2048 * (pg >> 1) + 64 * (4 * (pg & 1) + q4) + 16 * ((2 + (p4 >> 1)) ^ pg) + 8 * (p4 & 1);
    vA0 = v0 + 8192 * kh;
    vA1 = v1 + 8192 * kh;
    vB0 = v0 + 8192 * (1 - kh);
    vB1 = v1 + 8192 * (1 - kh);
  }

  if (g == 0 && tid < 128) {
    unsigned* sy = (unsigned*)((char*)o_ws + WS_SYNC) + tid * 32;
    sy[0] = 0u;
    sy[16] = 0u;
  }
  {
    const int u0 = tb ? 1 : 0;
    eissue(u0, 0);
    eissue(u0, 1);
    eissue(u0, 2);
  }
  issue(0);
  issue(1);
  issue(2);

  bf16x8 qf[2][4];
  bf16x8 qe[4];
  char* qpark = (char*)o_ws + WS_QPARK + (size_t)(g & 255) * 40960;

  auto qphase = [&](const int u, const int n0, const bool park) __attribute__((always_inline)) {
    STAMP(1);
    const int ln = lane_id();
    const int r16 = ln & 15, gq = ln >> 4, q4 = (ln & 15) >> 2, p4 = ln & 3;
    const unsigned wb0 = 2048 * gq + 64 * (0 + q4) + 16 * (p4 ^ ((2 * gq + 0) & 3));
    const unsigned wb1 = 2048 * gq + 64 * (4 + q4) + 16 * (p4 ^ ((2 * gq + 1) & 3));
    const unsigned we0 = wb0 + 512 * w, we1 = wb1 + 512 * w;
    const unsigned xb = 8192 + 64 * (32 * w + r16) + 16 * (gq ^ ((r16 >> 2) & 3));
    const unsigned xe0 = XCH + 128 * r16 + 16 * ((0 + gq) ^ ((r16 >> 1) & 7));
    const unsigned xe1 = XCH + 128 * r16 + 16 * ((4 + gq) ^ ((r16 >> 1) & 7));
    f32x4 aq[2][8] = {};
    f32x4 ae[2][2] = {};
    const __amdgpu_buffer_rsrc_t rsW = __builtin_amdgcn_make_buffer_rsrc((void*)(u ? Wp1 : Wp0), (short)0, DM * HD * 2, 0x00020000);
    const __amdgpu_buffer_rsrc_t rsX = __builtin_amdgcn_make_buffer_rsrc((void*)(u ? Xp1 : Xp0), (short)0, SQ * DM * 2, 0x00020000);
    const unsigned vW = mk_voffW(), vX = mk_voffX();
    const unsigned qdst = lds0 + (unsigned)w * 1024;
    auto qpiece = [&](auto C_, auto J_) __attribute__((always_inline)) {
      constexpr int cn = decltype(C_)::value, j = decltype(J_)::value;
      if constexpr (cn < 16) {
        if constexpr (j < 2) bdma<(cn & 3) * SLOT + 4096 * j, cn * (32 * 8192) + j * (16 * 8192), false>(rsW, vW, 0u, qdst);
        else bdma<(cn & 3) * SLOT + 4096 * j, 64 * cn + (j - 2) * 65536, false>(rsX, vX, 0u, qdst);
      } else {
        piece(j);
      }
    };
    static_for<16>([&](auto C_) __attribute__((always_inline)) {
      constexpr int c = decltype(C_)::value;
      WAIT_BAR(8);
      const lds_cptr sl = L + (c & 3) * SLOT;
      const lds_cptr sxe = L + 4096 * ((c >> 1) & 3) + ((c & 1) ? xe1 : xe0);
      if constexpr (c + 3 >= 16) prep(n0 + c + 3);
      bf16x8 xm[2], xe[2], af[10];
#define ARD(i) af[i] = ((i) < 8) ? tr2(sl + wb0 + 512 * ((i) >> 1) + 8 * ((i) & 1), sl + wb1 + 512 * ((i) >> 1) + 8 * ((i) & 1)) \
                                 : tr2(sl + we0 + 8 * ((i) & 1), sl + we1 + 8 * ((i) & 1))
      xm[0] = ldl(sl + xb);
      xm[1] = ldl(sl + xb + 1024);
      ARD(0);
      ARD(1);
      SB();
#pragma unroll
      for (int i = 0; i < 10; ++i) {
        if (i + 2 < 10) { ARD(i + 2); }
        if (i == 4) xe[0] = ldl(sxe);
        if (i == 5) xe[1] = ldl(sxe + 2048);
        if (i < 8) {
          aq[0][i] = MFMA16(af[i], xm[0], aq[0][i]);
          aq[1][i] = MFMA16(af[i], xm[1], aq[1][i]);
        } else {
          ae[0][i - 8] = MFMA16(af[i], xe[0], ae[0][i - 8]);
          ae[1][i - 8] = MFMA16(af[i], xe[1], ae[1][i - 8]);
        }
        if (i == 1) qpiece(std::integral_constant<int, c + 3>{}, std::integral_constant<int, 0>{});
        if (i == 3) qpiece(std::integral_constant<int, c + 3>{}, std::integral_constant<int, 1>{});
        if (i == 5) qpiece(std::integral_constant<int, c + 3>{}, std::integral_constant<int, 2>{});
        if (i == 7) qpiece(std::integral_constant<int, c + 3>{}, std::integral_constant<int, 3>{});
        SB();
      }
#undef ARD
      if ((c & 1) == 0) {
        if ((c >> 1) + 3 < 8) eissue(u, (c >> 1) + 3);
        else if (park) eissue(0, (c >> 1) + 3 - 8);
      }
    });
#pragma unroll
    for (int a = 0; a < 2; ++a)
#pragma unroll
      for (int s = 0; s < 4; ++s) qf[a][s] = pack44(aq[a][2 * s] * CSC, aq[a][2 * s + 1] * CSC);
    const bf16x8 e0 = pack44(ae[0][0] * CSC, ae[0][1] * CSC), e1 = pack44(ae[1][0] * CSC, ae[1][1] * CSC);
    if (park) {
#pragma unroll
      for (int a = 0; a < 2; ++a)
#pragma unroll
        for (int s = 0; s < 4; ++s) *(bf16x8*)(qpark + w * 8192 + 1024 * (4 * a + s) + 16 * ln) = qf[a][s];
      *(bf16x8*)(qpark + 32768 + 256 * r16 + 16 * (4 * w + gq)) = e0;
      *(bf16x8*)(qpark + 32768 + 256 * (16 + r16) + 16 * (4 * w + gq)) = e1;
    } else {
      *(__attribute__((address_space(3))) bf16x8*)(LW + XCH + 256 * r16 + 16 * ((4 * w + gq) ^ r16)) = e0;
      *(__attribute__((address_space(3))) bf16x8*)(LW + XCH + 256 * (16 + r16) + 16 * ((4 * w + gq) ^ r16)) = e1;
      LDS_BAR();
#pragma unroll
      for (int s = 0; s < 4; ++s) qe[s] = ldl(L + XCH + 256 * (16 * eg + r16) + 16 * ((4 * s + gq) ^ r16));
    }
    STAMP(6);
  };

  auto attention = [&](auto U_, const int nk) __attribute__((always_inline)) {
    constexpr int u = decltype(U_)::value;
    f32x4 o[2][8] = {};
    f32x4 oe[8] = {};
    float l0 = 0.f, l1 = 0.f, le = 0.f;
    f32x4 negm[2] = {};
    f32x4 negme = {};
    const __amdgpu_buffer_rsrc_t rsK = __builtin_amdgcn_make_buffer_rsrc((void*)(u ? Kp1 : Kp0), (short)0, SK * DH * 2, 0x00020000);
    const __amdgpu_buffer_rsrc_t rsV = __builtin_amdgcn_make_buffer_rsrc((void*)(u ? Vp1 : Vp0), (short)0, SK * DH * 2, 0x00020000);
    auto tile = [&](const int t, auto F_) __attribute__((always_inline)) {
      constexpr bool FAST = decltype(F_)::value;
      const unsigned so = (unsigned)t * 16384u;
      const unsigned fdst = lds0 + (unsigned)w * 1024 + ((unsigned)(t & 1) << 15);
      const unsigned vdst = lds0 + (unsigned)w * 1024 + 3 * SLOT - ((unsigned)(t & 1) << 15);
#define PIECE_V(j) do { if constexpr (FAST) bdma<4096 * (j), 16384 + 4096 * (j), true>(rsV, voffKV, so, vdst); else piece(j); } while (0)
#define PIECE_K(j) do { if constexpr (FAST) bdma<4096 * (j), 32768 + 4096 * (j), true>(rsK, voffKV, so, fdst); else piece(j); } while (0)
      STAMP(7);
      WAIT_BAR(8);
      STAMP(0);
      if constexpr (!FAST) {
        if (u == 0 && t == 14 && !tb) {
          eissue(1, 0);
          eissue(1, 1);
          eissue(1, 2);
        }
        prep(nk + 2 * t + 3);
      }
      const float thr = t == 0 ? -__builtin_inff() : THR_L2;
      const float lo = t == 0 ? -__builtin_inff() : 0.f;
      f32x4 s[2][4], se[2];
      u32x4 pw[2][2], pwe;
      float m0, m1, me;
#define EX(x) __builtin_amdgcn_exp2f(x)
#define P2(S, i, LL, DST) do { const float a_ = EX(S[i]), b_ = EX(S[(i) + 1]); LL += a_; LL += b_; unsigned d_ = cvtpk(a_, b_); PIN(d_); PIN(LL); DST = d_; } while (0)
      const lds_cptr ka = (lds_cptr)(uintptr_t)kA_, kb = (lds_cptr)(uintptr_t)kB_;
      bf16x8 kf[16];
#define KRD(i) kf[i] = ldl((((i) >> 3) ? kb : ka) + 4096 * (((i) >> 2) & 1) + 512 * ((i) & 3))
      KRD(0); KRD(1); KRD(2);
      SB();
#pragma unroll
      for (int i = 0; i < 8; ++i) {
        const int kt = i >> 2, ks = i & 3;
        KRD(i + 3);
        if (ks == 0) {
          s[0][kt] = mfma16_head(kf[i], qf[0][0], negm[0]);
          s[1][kt] = mfma16_head(kf[i], qf[1][0], negm[1]);
          se[kt] = mfma16_head(kf[i], qe[0], negme);
        } else {
          s[0][kt] = MFMA16(kf[i], qf[0][ks], s[0][kt]);
          s[1][kt] = MFMA16(kf[i], qf[1][ks], s[1][kt]);
          se[kt] = MFMA16(kf[i], qe[ks], se[kt]);
        }
        if (i == 1) PIECE_V(0);
        if (i == 5) PIECE_V(1);
        if (i == 5) { m0 = MX3(s[0][0][0], s[0][0][1], s[0][0][2]); m1 = MX3(s[1][0][0], s[1][0][1], s[1][0][2]); PIN(m0); PIN(m1); }
        if (i == 6) { me = MX3(se[0][0], se[0][1], se[0][2]); m0 = MX3(m0, s[0][0][3], m0); PIN(me); PIN(m0); }
        if (i == 7) { m1 = MX3(m1, s[1][0][3], m1); me = MX3(me, se[0][3], me); PIN(m1); PIN(me); }
        SB();
      }
      m0 = MX3(m0, s[0][1][0], s[0][1][1]); m0 = MX3(m0, s[0][1][2], s[0][1][3]);
      m1 = MX3(m1, s[1][1][0], s[1][1][1]); m1 = MX3(m1, s[1][1][2], s[1][1][3]);
      me = MX3(me, se[1][0], se[1][1]); me = MX3(me, se[1][2], se[1][3]);
      if (__builtin_expect(__any(MX3(m0, m1, me) > thr), 0)) {
        const float d0 = fmaxf(rowmax4(m0), lo), d1 = fmaxf(rowmax4(m1), lo), de = fmaxf(rowmax4(me), lo);
        const float a0 = EX(-fmaxf(d0, 0.f)), a1 = EX(-fmaxf(d1, 0.f)), ae_ = EX(-fmaxf(de, 0.f));
        l0 *= a0; l1 *= a1; le *= ae_;
#pragma unroll
        for (int d = 0; d < 8; ++d) { o[0][d] *= a0; o[1][d] *= a1; oe[d] *= ae_; }
        s[0][0] -= d0; s[0][1] -= d0; s[1][0] -= d1; s[1][1] -= d1; se[0] -= de; se[1] -= de;
        negm[0] -= d0; negm[1] -= d1; negme -= de;
        PIN(negm[0]); PIN(negm[1]); PIN(negme);
        asm volatile("s_nop 1");
      }
      SB();
#pragma unroll
      for (int i = 0; i < 8; ++i) {
        const int kt = 2 + (i >> 2), ks = i & 3;
        if (i + 11 < 16) { KRD(i + 11); }
        if (ks == 0) {
          s[0][kt] = mfma16_head(kf[8 + i], qf[0][0], negm[0]);
          s[1][kt] = mfma16_head(kf[8 + i], qf[1][0], negm[1]);
        } else {
          s[0][kt] = MFMA16(kf[8 + i], qf[0][ks], s[0][kt]);
          s[1][kt] = MFMA16(kf[8 + i], qf[1][ks], s[1][kt]);
        }
        if (i < 4) P2(s[0][i >> 1], 2 * (i & 1), l0, pw[0][0][i]);
        else P2(s[1][(i - 4) >> 1], 2 * (i & 1), l1, pw[1][0][i - 4]);
        if (i & 1) P2(se[i >> 2], 2 * ((i >> 1) & 1), le, pwe[i >> 1]);
        if (i == 2) PIECE_V(2);
        if (i == 6) PIECE_V(3);
        SB();
      }
#undef KRD
      m0 = MX3(s[0][2][0], s[0][2][1], s[0][2][2]); m0 = MX3(m0, s[0][2][3], s[0][3][0]); m0 = MX3(m0, s[0][3][1], s[0][3][2]);
      m1 = MX3(s[1][2][0], s[1][2][1], s[1][2][2]); m1 = MX3(m1, s[1][2][3], s[1][3][0]); m1 = MX3(m1, s[1][3][1], s[1][3][2]);
      m0 = MX3(m0, s[0][3][3], m0); m1 = MX3(m1, s[1][3][3], m1);
      if (__builtin_expect(__any(MX3(m0, m1, m1) > THR_L2), 0)) {
        const float d0 = fmaxf(rowmax4(m0), 0.f), d1 = fmaxf(rowmax4(m1), 0.f);
        const float a0 = EX(-d0), a1 = EX(-d1);
        l0 *= a0; l1 *= a1;
#pragma unroll
        for (int d = 0; d < 8; ++d) { o[0][d] *= a0; o[1][d] *= a1; }
#pragma unroll
        for (int i = 0; i < 4; ++i) {
          const unsigned v0 = pw[0][0][i], v1 = pw[1][0][i];
          pw[0][0][i] = cvtpk(__uint_as_float(v0 << 16) * a0, __uint_as_float(v0 & 0xffff0000u) * a0);
          pw[1][0][i] = cvtpk(__uint_as_float(v1 << 16) * a1, __uint_as_float(v1 & 0xffff0000u) * a1);
        }
        s[0][2] -= d0; s[0][3] -= d0; s[1][2] -= d1; s[1][3] -= d1;
        negm[0] -= d0; negm[1] -= d1;
      }
      STAMP(2);
      WAIT_BAR(8);
      STAMP(3);
      if constexpr (!FAST) prep(nk + 2 * t + 4);
      {
        const lds_cptr a0 = (lds_cptr)(uintptr_t)vA0, a1 = (lds_cptr)(uintptr_t)vA1, b0 = (lds_cptr)(uintptr_t)vB0, b1 = (lds_cptr)(uintptr_t)vB1;
        bf16x8 vf[16];
#define VB(i) (((i) >> 3) ? (((i) & 1) ? b1 : b0) : (((i) & 1) ? a1 : a0))
#define VRD(i) vf[i] = tr2(VB(i) + 512 * (((i) & 7) >> 1), VB(i) + 512 * (((i) & 7) >> 1) + 4096)
        VRD(0); VRD(1);
        SB();
#pragma unroll
        for (int i = 0; i < 8; ++i) {
          VRD(i + 2);
          o[0][i] = MFMA16(vf[i], __builtin_bit_cast(bf16x8, pw[0][0]), o[0][i]);
          o[1][i] = MFMA16(vf[i], __builtin_bit_cast(bf16x8, pw[1][0]), o[1][i]);
          oe[i] = MFMA16(vf[i], __builtin_bit_cast(bf16x8, pwe), oe[i]);
          if (i < 4) P2(s[0][2 + (i >> 1)], 2 * (i & 1), l0, pw[0][1][i]);
          else P2(s[1][2 + ((i - 4) >> 1)], 2 * (i & 1), l1, pw[1][1][i - 4]);
          if (i == 1) PIECE_K(0);
          if (i == 5) PIECE_K(1);
          SB();
        }
#pragma unroll
        for (int i = 0; i < 8; ++i) {
          if (i + 10 < 16) { VRD(i + 10); }
          o[0][i] = MFMA16(vf[8 + i], __builtin_bit_cast(bf16x8, pw[0][1]), o[0][i]);
          o[1][i] = MFMA16(vf[8 + i], __builtin_bit_cast(bf16x8, pw[1][1]), o[1][i]);
          if (i == 1) PIECE_K(2);
          if (i == 5) PIECE_K(3);
          SB();
        }
#undef VB
#undef VRD
      }
#undef EX
#undef P2
      STAMP(5);
      const unsigned dir = (t & 1) ? 0u - 2 * SLOT : 2 * SLOT;
      kA_ += dir; kB_ += dir; vA0 += dir; vA1 += dir; vB0 += dir; vB1 += dir;
      PIN(kA_); PIN(kB_); PIN(vA0); PIN(vA1); PIN(vB0); PIN(vB1);
#undef PIECE_V
#undef PIECE_K
    };
#pragma unroll 1
    for (int t = 0; t < 14; ++t) tile(t, std::true_type{});
#pragma unroll 1
    for (int t = 14; t < 16; ++t) tile(t, std::false_type{});

    char* ob = (char*)o_ws + (size_t)((u * NB + b) * NH + h) * (40 * 1024);
    const int ln = lane_id();
    int w = w_;
    asm volatile("" : "+s"(w));
    const int eg = w & 1, kh = w >> 1;
    {
      const float i0 = 1.0f / rowsum4(l0), i1 = 1.0f / rowsum4(l1);
      char* of = ob + w * 8192 + 16 * ln;
#pragma unroll
      for (int uu = 0; uu < 4; ++uu) {
        *(bf16x8*)(of + 1024 * uu) = pack44(o[0][2 * uu] * i0, o[0][2 * uu + 1] * i0);
        *(bf16x8*)(of + 4096 + 1024 * uu) = pack44(o[1][2 * uu] * i1, o[1][2 * uu + 1] * i1);
      }
    }
    {
      constexpr unsigned EX0 = 3 * SLOT;
      const int r16 = ln & 15;
      const float me = -negme[0];
      le = rowsum4(le);
      LDS_BAR();
      if (ln < 16) {
        f32x2 ml = {me, le};
        *(__attribute__((address_space(3))) f32x2*)(LW + EX0 + 4096 * (w ^ 2) + 8 * ln) = ml;
      }
      LDS_BAR();
      f32x2 pml = *(const __attribute__((address_space(3))) f32x2*)(L + EX0 + 4096 * w + 8 * r16);
      const float M = fmaxf(me, pml[0]);
      const float sc = __builtin_amdgcn_exp2f(me - M), scp = __builtin_amdgcn_exp2f(pml[0] - M);
      const float f = sc / (le * sc + pml[1] * scp);
      asm volatile("s_waitcnt lgkmcnt(0)" ::: "memory");
      f32x4 keep[4];
#pragma unroll
      for (int i = 0; i < 4; ++i) {
        f32x4 snd;
#pragma unroll
        for (int r = 0; r < 4; ++r) {
          snd[r] = (kh ? oe[i][r] : oe[4 + i][r]) * f;
          keep[i][r] = (kh ? oe[4 + i][r] : oe[i][r]) * f;
        }
        *(__attribute__((address_space(3))) f32x4*)(LW + EX0 + 4096 * w + 1024 * i + 16 * ln) = snd;
      }
      LDS_BAR();
#pragma unroll
      for (int i = 0; i < 4; ++i) keep[i] += *(const __attribute__((address_space(3))) f32x4*)(L + EX0 + 4096 * (w ^ 2) + 1024 * i + 16 * ln);
      char* ef = ob + 32768 + 4096 * eg + 2048 * kh + 16 * ln;
      *(bf16x8*)(ef) = pack44(keep[0], keep[1]);
      *(bf16x8*)(ef + 1024) = pack44(keep[2], keep[3]);
    }
  };

#pragma unroll 1
  for (int i = 0; i < (tb ? 2 : 1); ++i) qphase((tb && i == 0) ? 1 : 0, 16 * i, tb && i == 0);
  attention(std::integral_constant<int, 0>{}, tb ? 32 : 16);
  __builtin_amdgcn_s_setprio(0);
  if (tb) {
    const int ln = lane_id();
#pragma unroll
    for (int a = 0; a < 2; ++a)
#pragma unroll
      for (int s = 0; s < 4; ++s) qf[a][s] = *(const bf16x8*)(qpark + w * 8192 + 1024 * (4 * a + s) + 16 * ln);
#pragma unroll
    for (int s = 0; s < 4; ++s) qe[s] = *(const bf16x8*)(qpark + 32768 + 256 * (16 * eg + (ln & 15)) + 16 * (4 * s + (ln >> 4)));
#pragma unroll
    for (int a = 0; a < 2; ++a)
#pragma unroll
      for (int s = 0; s < 4; ++s) PIN(qf[a][s]);
#pragma unroll
    for (int s = 0; s < 4; ++s) PIN(qe[s]);
  } else {
    qphase(1, 48, false);
  }
  attention(std::integral_constant<int, 1>{}, 64);
  asm volatile("s_waitcnt vmcnt(0)" ::: "memory");
}

constexpr unsigned S2 = 36864, NS2 = 4, LDS2 = S2 * NS2;

__global__ __launch_bounds__(512, 2) void oproj_kernel(char* __restrict__ ws, const u16* __restrict__ woA, const u16* __restrict__ woB,
                                                       u16* __restrict__ out) {
  extern __shared__ __attribute__((aligned(16))) char smem[];
  const int tid = threadIdx.x, lane = tid & 63;
  const int w = __builtin_amdgcn_readfirstlane(tid >> 6);
  const int wn = w & 3, wk = w >> 2;
  const int g = blockIdx.x, xcd = g & 7, idx = g >> 3;
  const int blkk = xcd >> 2, mt = 4 * (xcd & 3) + (idx >> 3), nt = (idx >> 1) & 3, kh = idx & 1;
  const int tile = (blkk * 16 + mt) * 4 + nt;
  const unsigned lds0 = (unsigned)(uintptr_t)smem;
  const lds_cptr L = (lds_cptr)smem;
  const lds_ptr LW = (lds_ptr)smem;
  const int r16 = lane & 15, gq = lane >> 4, q4 = (lane & 15) >> 2, p4 = lane & 3;
  const char* src0;
  unsigned voff, dst0;
  if (wk == 0) {
    src0 = (const char*)ws + (size_t)((blkk * NB + mt) * NH + kh * 16) * (40 * 1024) + 4096 * (wn >> 1) + 1024 * (wn & 1);
    voff = 16 * lane;
    dst0 = lds0 + (unsigned)wn * 1024;
  } else {
    src0 = (const char*)((blkk ? woB : woA) + (size_t)(kh * 2048) * DM + nt * 128);
    voff = (unsigned)(8 * (wn >> 1) + ((lane >> 2) & 7)) * (DM * 2) + 128 * (wn & 1) + 64 * (lane >> 5) + 16 * ((lane & 3) ^ (2 * ((lane >> 4) & 1)));
    dst0 = lds0 + 20480 + (unsigned)wn * 1024;
  }
  const __amdgpu_buffer_rsrc_t rsd = __builtin_amdgcn_make_buffer_rsrc((void*)src0, (short)0, 4 << 20, 0x00020000);
  unsigned c_so, c_dst;
  auto cprep = [&](int c) {
    const unsigned cc = (unsigned)c & 31u;
    c_so = wk == 0 ? (cc >> 1) * 40960u + (cc & 1) * 2048u : cc * (64u * DM * 2);
    c_dst = dst0 + (unsigned)(c & 3) * S2;
  };
#define PIECE2(j) do { if (wk == 0) bdma<4096 * (j), 8192 * (j), false>(rsd, voff, c_so, c_dst); else if ((j) < 4) bdma<4096 * (j), 16 * DM * 2 * (j), false>(rsd, voff, c_so, c_dst); } while (0)
  const unsigned ob0 = 1024 * wk + 16 * lane;
  const unsigned wb0 = 20480 + 2048 * (4 * wk + (gq >> 1)) + 512 * wn + 64 * (4 * (gq & 1) + q4) + 32 * (0 ^ (gq & 1)) + 8 * p4;
  const unsigned wb1 = 20480 + 2048 * (4 * wk + (gq >> 1)) + 512 * wn + 64 * (4 * (gq & 1) + q4) + 32 * (1 ^ (gq & 1)) + 8 * p4;
#define DUMP()
  f32x4 acc[2][10] = {};
  bf16x8 fa[2][2], fb[2][10];
  auto rdA = [&](int buf, const lds_cptr sl, int n2) { fa[buf][n2] = tr2(sl + (n2 ? wb1 : wb0), sl + (n2 ? wb1 : wb0) + 4096); };
  auto rdB = [&](int buf, const lds_cptr sl, int i) { fb[buf][i] = ldl(sl + ob0 + 2048 * i); };
#pragma unroll
  for (int c = 0; c < 4; ++c) {
    cprep(c);
    PIECE2(0); PIECE2(1); PIECE2(2); PIECE2(3); PIECE2(4);
  }
  if (wk == 0) WAIT_BAR(15);
  else WAIT_BAR(12);
  rdA(0, L, 0);
  rdA(0, L, 1);
#pragma unroll
  for (int i = 0; i < 10; ++i) rdB(0, L, i);
  auto step = [&](int c, auto B_) __attribute__((always_inline)) {
    constexpr int bf = decltype(B_)::value;
    if (wk == 0) WAIT_BAR(10);
    else WAIT_BAR(8);
    const lds_cptr sl = L + ((c + 1) & 3) * S2;
    cprep(c + 4);
    SB();
#pragma unroll
    for (int k = 0; k < 10; ++k) {
      if (k == 0) { rdA(bf ^ 1, sl, 0); rdA(bf ^ 1, sl, 1); }
      if (k >= 1 && k <= 5) { rdB(bf ^ 1, sl, 2 * k - 2); rdB(bf ^ 1, sl, 2 * k - 1); }
      acc[0][k] = MFMA16(fa[bf][0], fb[bf][k], acc[0][k]);
      acc[1][k] = MFMA16(fa[bf][1], fb[bf][k], acc[1][k]);
      if (k == 1) PIECE2(0);
      if (k == 3) PIECE2(1);
      if (k == 5) PIECE2(2);
      if (k == 7) PIECE2(3);
      if (k == 9) PIECE2(4);
      SB();
    }
  };
  STAMP(0);
#pragma unroll 1
  for (int c = 0; c < 32; c += 2) {
    step(c, std::integral_constant<int, 0>{});
    step(c + 1, std::integral_constant<int, 1>{});
  }
  STAMP(1);
  WAIT_BAR(0);
  f32x4 res[10];
  auto red = [&](auto K_) __attribute__((always_inline)) {
    constexpr int kk = decltype(K_)::value;
#pragma unroll
    for (int i = 0; i < 10; ++i) *(__attribute__((address_space(3))) f32x4*)(LW + 10240 * w + 1024 * i + 16 * lane) = acc[1 - kk][i];
    LDS_BAR();
#pragma unroll
    for (int i = 0; i < 10; ++i) res[i] = acc[kk][i] + *(const __attribute__((address_space(3))) f32x4*)(L + 10240 * (w ^ 4) + 1024 * i + 16 * lane);
  };
  if (wk) red(std::integral_constant<int, 1>{});
  else red(std::integral_constant<int, 0>{});
  STAMP(2);
  unsigned* sync = (unsigned*)(ws + WS_SYNC + (size_t)tile * 128);
  volatile __attribute__((address_space(3))) unsigned* tk = (volatile __attribute__((address_space(3))) unsigned*)(LW + 90112);
  if (tid == 0) *tk = __hip_atomic_fetch_add(sync, 1u, __ATOMIC_RELAXED, __HIP_MEMORY_SCOPE_AGENT);
  LDS_BAR();
  const unsigned ticket = __builtin_amdgcn_readfirstlane(*tk);
  const __amdgpu_buffer_rsrc_t rs = __builtin_amdgcn_make_buffer_rsrc(ws + WS_PART + (size_t)tile * 81920, 0, 81920, 0x00020000);
  const unsigned poff = (unsigned)w * 10240 + 16 * lane;
  if (ticket == 0) {
#pragma unroll
    for (int i = 0; i < 10; ++i) __builtin_amdgcn_raw_buffer_store_b128(__builtin_bit_cast(u32x4, res[i]), rs, poff + 1024 * i, 0, 16);
    asm volatile("s_waitcnt vmcnt(0)" ::: "memory");
    __builtin_amdgcn_s_barrier();
    if (tid == 0) __hip_atomic_store(sync + 16, 1u, __ATOMIC_RELAXED, __HIP_MEMORY_SCOPE_AGENT);
    STAMP(3);
    DUMP();
    return;
  }
  if (tid == 0) {
    while (__hip_atomic_load(sync + 16, __ATOMIC_RELAXED, __HIP_MEMORY_SCOPE_AGENT) == 0u) __builtin_amdgcn_s_sleep(1);
    __builtin_amdgcn_fence(__ATOMIC_ACQUIRE, "agent");
    asm volatile("s_waitcnt vmcnt(0)" ::: "memory");
  }
  __syncthreads();
  STAMP(4);
#pragma unroll
  for (int i = 0; i < 10; ++i) {
    u16* orow = out + (size_t)(mt * 160 + 16 * i + r16) * 1024 + blkk * 512 + nt * 128 + 32 * wn + 16 * wk + 4 * gq;
    const f32x4 v = __builtin_bit_cast(f32x4, __builtin_amdgcn_raw_buffer_load_b128(rs, poff + 1024 * i, 0, 16));
    u32x2 pv = {cvtpk(res[i][0] + v[0], res[i][1] + v[1]), cvtpk(res[i][2] + v[2], res[i][3] + v[3])};
    *(u32x2*)orow = pv;
  }
  STAMP(5);
  DUMP();
}

extern "C" void kernel_launch(void* const* d_in, const int* in_sizes, int n_in, void* d_out, int out_size, void* d_ws, size_t ws_size,
                              hipStream_t stream) {
  const u16* xA = (const u16*)d_in[0];
  const u16* kA = (const u16*)d_in[1];
  const u16* vA = (const u16*)d_in[2];
  const u16* wqA = (const u16*)d_in[3];
  const u16* woA = (const u16*)d_in[4];
  const u16* xB = (const u16*)d_in[5];
  const u16* kB = (const u16*)d_in[6];
  const u16* vB = (const u16*)d_in[7];
  const u16* wqB = (const u16*)d_in[8];
  const u16* woB = (const u16*)d_in[9];
  u16* o_ws = (u16*)d_ws;
  (void)hipFuncSetAttribute((const void*)attn_kernel, hipFuncAttributeMaxDynamicSharedMemorySize, (int)LDS1);
  (void)hipFuncSetAttribute((const void*)oproj_kernel, hipFuncAttributeMaxDynamicSharedMemorySize, (int)LDS2);
  hipLaunchKernelGGL(attn_kernel, dim3(512), dim3(256), LDS1, stream, xA, kA, vA, wqA, xB, kB, vB, wqB, o_ws);
  hipLaunchKernelGGL(oproj_kernel, dim3(256), dim3(512), LDS2, stream, (char*)d_ws, woA, woB, (u16*)d_out);
}
```

```cpp
#define SPIN_MAX 1u
#include <hip/hip_runtime.h>
#include <hip/hip_bf16.h>
#include <stdint.h>
#include <type_traits>
#include <utility>

typedef short bf16x8 __attribute__((ext_vector_type(8)));
typedef short s16x4 __attribute__((ext_vector_type(4)));
typedef float f32x4 __attribute__((ext_vector_type(4)));
typedef float f32x2 __attribute__((ext_vector_type(2)));
typedef __bf16 bf16x2_t __attribute__((ext_vector_type(2)));
typedef unsigned u32x4 __attribute__((ext_vector_type(4)));
typedef unsigned u32x2 __attribute__((ext_vector_type(2)));
typedef unsigned short u16;
typedef __attribute__((address_space(3))) char* lds_ptr;
typedef __attribute__((address_space(3))) const char* lds_cptr;
typedef short v4i16_t __attribute__((ext_vector_type(4)));

#define STAMP(i)
#define DI __device__ __forceinline__
#define MFMA16(a, b, c) __builtin_amdgcn_mfma_f32_16x16x32_bf16((a), (b), (c), 0, 0, 0)
#define STR2(x) #x
#define STR(x) STR2(x)
#define WAIT_BAR(N) asm volatile("s_waitcnt vmcnt(" STR(N) ") lgkmcnt(0)\n\ts_barrier" ::: "memory")
#define LDS_BAR() asm volatile("s_waitcnt lgkmcnt(0)\n\ts_barrier" ::: "memory")
#define SB() __builtin_amdgcn_sched_barrier(0)
#define MX3(a, b, c) __builtin_fmaxf(__builtin_fmaxf((a), (b)), (c))
#define PIN(x) asm volatile("" : "+v"(x))

constexpr int SQ = 160, SK = 1024, DH = 128, NH = 32, NB = 16, DM = 512, HD = 4096;
constexpr float CSC = 0.088388347648318447f * 1.4426950408889634f;
constexpr float THR_L2 = 8.0f;

template <int N, class F, int... I>
DI void static_for_impl(F&& f, std::integer_sequence<int, I...>) { (f(std::integral_constant<int, I>{}), ...); }
template <int N, class F>
DI void static_for(F&& f) { static_for_impl<N>(f, std::make_integer_sequence<int, N>{}); }

DI unsigned cvtpk(float lo, float hi) {
  f32x2 v = {lo, hi};
  bf16x2_t b = __builtin_convertvector(v, bf16x2_t);
  return __builtin_bit_cast(unsigned, b);
}
DI bf16x8 pack44(const f32x4& a, const f32x4& b) {
  u32x4 w = {cvtpk(a[0], a[1]), cvtpk(a[2], a[3]), cvtpk(b[0], b[1]), cvtpk(b[2], b[3])};
  return __builtin_bit_cast(bf16x8, w);
}
DI s16x4 trd(lds_cptr p) {
  return __builtin_bit_cast(s16x4, __builtin_amdgcn_ds_read_tr16_b64_v4i16((__attribute__((address_space(3))) v4i16_t*)p));
}
DI bf16x8 tr2(lds_cptr p0, lds_cptr p1) {
  s16x4 a = trd(p0), b = trd(p1);
  return __builtin_shufflevector(a, b, 0, 1, 2, 3, 4, 5, 6, 7);
}
DI bf16x8 ldl(lds_cptr p) { return *(const __attribute__((address_space(3))) bf16x8*)p; }
DI int lane_id() {
  int l;
  asm volatile("v_mbcnt_lo_u32_b32 %0, -1, 0\n\tv_mbcnt_hi_u32_b32 %0, -1, %0" : "=v"(l));
  return l;
}
DI void dma16(const char* sbase, unsigned voff, unsigned lds_dst) {
  asm volatile("s_mov_b32 m0, %2\n\ts_nop 0\n\tglobal_load_lds_dwordx4 %1, %0" ::"s"(sbase), "v"(voff), "s"(lds_dst) : "memory");
}
DI void dma16s(const char* sbase, unsigned voff, unsigned lds_dst) {
  asm volatile("s_mov_b32 m0, %2\n\ts_nop 0\n\tglobal_load_lds_dwordx4 %1, %0 nt" ::"s"(sbase), "v"(voff), "s"(lds_dst) : "memory");
}
template <int IM, int IMG, bool NT>
DI void bdma(__amdgpu_buffer_rsrc_t rs, unsigned voff, unsigned so, unsigned dst) {
  unsigned tmp;
  if constexpr (NT)
    asm volatile("s_add_u32 m0, %[dst], %[im]\n\ts_add_u32 %[tmp], %[so], %[ig]\n\tbuffer_load_dwordx4 %[v], %[rs], %[tmp] offen nt lds"
                 : [tmp] "=&s"(tmp)
                 : [dst] "s"(dst), [so] "s"(so), [im] "i"(IM), [ig] "i"(IMG), [v] "v"(voff), [rs] "s"(rs)
                 : "memory", "scc");
  else
    asm volatile("s_add_u32 m0, %[dst], %[im]\n\ts_add_u32 %[tmp], %[so], %[ig]\n\tbuffer_load_dwordx4 %[v], %[rs], %[tmp] offen lds"
                 : [tmp] "=&s"(tmp)
                 : [dst] "s"(dst), [so] "s"(so), [im] "i"(IM), [ig] "i"(IMG), [v] "v"(voff), [rs] "s"(rs)
                 : "memory", "scc");
}
DI float rowmax4(float x) {
  auto r = __builtin_amdgcn_permlane16_swap(__float_as_uint(x), __float_as_uint(x), false, false);
  x = fmaxf(__uint_as_float(r[0]), __uint_as_float(r[1]));
  auto s = __builtin_amdgcn_permlane32_swap(__float_as_uint(x), __float_as_uint(x), false, false);
  return fmaxf(__uint_as_float(s[0]), __uint_as_float(s[1]));
}
DI float rowsum4(float x) {
  auto r = __builtin_amdgcn_permlane16_swap(__float_as_uint(x), __float_as_uint(x), false, false);
  x = __uint_as_float(r[0]) + __uint_as_float(r[1]);
  auto s = __builtin_amdgcn_permlane32_swap(__float_as_uint(x), __float_as_uint(x), false, false);
  return __uint_as_float(s[0]) + __uint_as_float(s[1]);
}
DI f32x4 mfma16_head(bf16x8 a, bf16x8 b, const f32x4& c) {
  f32x4 d;
  asm volatile("v_mfma_f32_16x16x32_bf16 %0, %1, %2, %3" : "=&v"(d) : "v"(a), "v"(b), "v"(c));
  return d;
}

constexpr unsigned SLOT = 16384, XCH = 65536, LDS1 = 81920;
constexpr size_t WS_SYNC = (size_t)2 * NB * SQ * HD * 2;
constexpr size_t WS_PART = WS_SYNC + 128 * 128;
constexpr size_t WS_QPARK = (size_t)64 << 20;

__global__ __launch_bounds__(256, 2) void attn_kernel(const u16* __restrict__ xA, const u16* __restrict__ kA, const u16* __restrict__ vA,
                                                      const u16* __restrict__ wqA, const u16* __restrict__ xB, const u16* __restrict__ kB,
                                                      const u16* __restrict__ vB, const u16* __restrict__ wqB, u16* __restrict__ o_ws) {
  extern __shared__ __attribute__((aligned(16))) char smem[];
  const int tid = threadIdx.x, lane = tid & 63;
  const int w = __builtin_amdgcn_readfirstlane(tid >> 6);
  const int g = blockIdx.x, xcd = g & 7, idx = g >> 3;
  const int h = 4 * xcd + (idx & 3), b = idx >> 2;
  const unsigned lds0 = (unsigned)(uintptr_t)smem;
  const lds_cptr L = (lds_cptr)smem;
  const lds_ptr LW = (lds_ptr)smem;

  const size_t kvoff = (size_t)(b * NH + h) * SK * DH * 2;
  const char* Kp0 = (const char*)kA + kvoff;
  const char* Vp0 = (const char*)vA + kvoff;
  const char* Kp1 = (const char*)kB + kvoff;
  const char* Vp1 = (const char*)vB + kvoff;
  const char* Wp0 = (const char*)wqA + h * 256;
  const char* Wp1 = (const char*)wqB + h * 256;
  const char* Xp0 = (const char*)(xA + (size_t)b * SQ * DM);
  const char* Xp1 = (const char*)(xB + (size_t)b * SQ * DM);

  const unsigned rowl = 8 * (w >> 1) + ((lane >> 2) & 7);
  const unsigned chs = 8 * (w & 1) + 4 * (lane >> 5) + ((lane & 3) ^ (2 * (w >> 1) + ((lane >> 4) & 1)));
  const unsigned voffKV = rowl * 256 + chs * 16;
  auto mk_voffE = [&]() {
    const int ln = lane_id();
    return (unsigned)(8 * w + (ln >> 3)) * (DM * 2) + 16 * ((ln & 7) ^ (4 * (w & 1) + (ln >> 4)));
  };
  auto mk_voffX = [&]() {
    const int ln = lane_id();
    return (unsigned)(16 * w + (ln >> 2)) * (DM * 2) + 16 * ((ln & 3) ^ ((ln >> 4) & 3));
  };
  auto mk_voffW = [&]() {
    unsigned v = voffKV;
    asm volatile("" : "+v"(v));
    return ((v & ~255u) << 5) | (v & 255u);
  };

  const bool tb = g >= 256;
  const int w_ = w;
  if (tb) __builtin_amdgcn_s_setprio(1);

  const char *d_src, *d_src2;
  unsigned d_dst;
  bool d_kv;
  auto prep = [&](int nn) {
    d_dst = lds0 + (unsigned)(nn & 3) * SLOT + (unsigned)w * 1024;
    const int ph = nn >> 4, j = nn & 15;
    const bool isq = tb ? (ph < 2 || ph >= 6) : (ph == 0 || ph == 3 || ph >= 6);
    const int un = tb ? (ph == 0 || ph == 4 || ph == 5) : (ph >= 3 && ph < 6);
    const int hf = tb ? (ph & 1) : (ph == 2 || ph == 5);
    d_kv = !isq;
    if (isq) {
      d_src = (un ? Wp1 : Wp0) + (size_t)j * (32 * 8192);
      d_src2 = (un ? Xp1 : Xp0) + 64 * j;
    } else {
      d_src = ((j & 1) ? (un ? Vp1 : Vp0) : (un ? Kp1 : Kp0)) + (size_t)(8 * hf + (j >> 1)) * 16384;
      d_src2 = d_src;
    }
  };
  auto piece = [&](int j) {
    const unsigned dst = d_dst + 4096u * (unsigned)j;
    if (d_kv) dma16s(d_src + 4096 * j, voffKV, dst);
    else if (j < 2) dma16(d_src + (size_t)j * (16 * 8192), mk_voffW(), dst);
    else dma16(d_src2 + (size_t)(j - 2) * (64 * 1024), mk_voffX(), dst);
  };
  auto issue = [&](int nn) {
    prep(nn);
    piece(0);
    piece(1);
    piece(2);
    piece(3);
  };
  auto eissue = [&](int u, int e) { dma16((u ? Xp1 : Xp0) + 128 * 1024 + 128 * e, mk_voffE(), lds0 + XCH + 4096u * (unsigned)(e & 3) + (unsigned)w * 1024); };

  const int eg = w & 1, kh = w >> 1;
  unsigned kA_, kB_, vA0, vA1, vB0, vB1;
  {
    const int r16 = lane & 15, gq = lane >> 4, q4 = (lane & 15) >> 2, p4 = lane & 3;
    const int pa = (0x78 >> (2 * (r16 >> 2))) & 3, pg = (0x78 >> (2 * gq)) & 3;
    const unsigned kb = lds0 + 2048 * (pa >> 1) + 64 * (4 * (pa & 1) + (r16 & 3)) + 16 * (gq ^ pa);
    kA_ = kb + 8192 * kh;
    kB_ = kb + 8192 * (1 - kh);
    const unsigned v0 = lds0 + SLOT + 2048 * (pg >> 1) + 64 * (4 * (pg & 1) + q4) + 16 * ((0 + (p4 >> 1)) ^ pg) + 8 * (p4 & 1);
    const unsigned v1 = lds0 + SLOT + 2048 * (pg >> 1) + 64 * (4 * (pg & 1) + q4) + 16 * ((2 + (p4 >> 1)) ^ pg) + 8 * (p4 & 1);
    vA0 = v0 + 8192 * kh;
    vA1 = v1 + 8192 * kh;
    vB0 = v0 + 8192 * (1 - kh);
    vB1 = v1 + 8192 * (1 - kh);
  }

  if (g == 0 && tid < 128) {
    unsigned* sy = (unsigned*)((char*)o_ws + WS_SYNC) + tid * 32;
    sy[0] = 0u;
    sy[1] = 0u;
    sy[16] = 0u;
    sy[17] = 0u;
  }
  {
    const int u0 = tb ? 1 : 0;
    eissue(u0, 0);
    eissue(u0, 1);
    eissue(u0, 2);
  }
  issue(0);
  issue(1);
  issue(2);

  bf16x8 qf[2][4];
  bf16x8 qe[4];
  char* qpark = (char*)o_ws + WS_QPARK + (size_t)(g & 255) * 40960;

  auto qphase = [&](const int u, const int n0, const bool park) __attribute__((always_inline)) {
    STAMP(1);
    const int ln = lane_id();
    const int r16 = ln & 15, gq = ln >> 4, q4 = (ln & 15) >> 2, p4 = ln & 3;
    const unsigned wb0 = 2048 * gq + 64 * (0 + q4) + 16 * (p4 ^ ((2 * gq + 0) & 3));
    const unsigned wb1 = 2048 * gq + 64 * (4 + q4) + 16 * (p4 ^ ((2 * gq + 1) & 3));
    const unsigned we0 = wb0 + 512 * w, we1 = wb1 + 512 * w;
    const unsigned xb = 8192 + 64 * (32 * w + r16) + 16 * (gq ^ ((r16 >> 2) & 3));
    const unsigned xe0 = XCH + 128 * r16 + 16 * ((0 + gq) ^ ((r16 >> 1) & 7));
    const unsigned xe1 = XCH + 128 * r16 + 16 * ((4 + gq) ^ ((r16 >> 1) & 7));
    f32x4 aq[2][8] = {};
    f32x4 ae[2][2] = {};
    const __amdgpu_buffer_rsrc_t rsW = __builtin_amdgcn_make_buffer_rsrc((void*)(u ? Wp1 : Wp0), (short)0, DM * HD * 2, 0x00020000);
    const __amdgpu_buffer_rsrc_t rsX = __builtin_amdgcn_make_buffer_rsrc((void*)(u ? Xp1 : Xp0), (short)0, SQ * DM * 2, 0x00020000);
    const unsigned vW = mk_voffW(), vX = mk_voffX();
    const unsigned qdst = lds0 + (unsigned)w * 1024;
    auto qpiece = [&](auto C_, auto J_) __attribute__((always_inline)) {
      constexpr int cn = decltype(C_)::value, j = decltype(J_)::value;
      if constexpr (cn < 16) {
        if constexpr (j < 2) bdma<(cn & 3) * SLOT + 4096 * j, cn * (32 * 8192) + j * (16 * 8192), false>(rsW, vW, 0u, qdst);
        else bdma<(cn & 3) * SLOT + 4096 * j, 64 * cn + (j - 2) * 65536, false>(rsX, vX, 0u, qdst);
      } else {
        piece(j);
      }
    };
    static_for<16>([&](auto C_) __attribute__((always_inline)) {
      constexpr int c = decltype(C_)::value;
      WAIT_BAR(8);
      const lds_cptr sl = L + (c & 3) * SLOT;
      const lds_cptr sxe = L + 4096 * ((c >> 1) & 3) + ((c & 1) ? xe1 : xe0);
      if constexpr (c + 3 >= 16) prep(n0 + c + 3);
      bf16x8 xm[2], xe[2], af[10];
#define ARD(i) af[i] = ((i) < 8) ? tr2(sl + wb0 + 512 * ((i) >> 1) + 8 * ((i) & 1), sl + wb1 + 512 * ((i) >> 1) + 8 * ((i) & 1)) \
                                 : tr2(sl + we0 + 8 * ((i) & 1), sl + we1 + 8 * ((i) & 1))
      xm[0] = ldl(sl + xb);
      xm[1] = ldl(sl + xb + 1024);
      ARD(0);
      ARD(1);
      SB();
#pragma unroll
      for (int i = 0; i < 10; ++i) {
        if (i + 2 < 10) { ARD(i + 2); }
        if (i == 4) xe[0] = ldl(sxe);
        if (i == 5) xe[1] = ldl(sxe + 2048);
        if (i < 8) {
          aq[0][i] = MFMA16(af[i], xm[0], aq[0][i]);
          aq[1][i] = MFMA16(af[i], xm[1], aq[1][i]);
        } else {
          ae[0][i - 8] = MFMA16(af[i], xe[0], ae[0][i - 8]);
          ae[1][i - 8] = MFMA16(af[i], xe[1], ae[1][i - 8]);
        }
        if (i == 1) qpiece(std::integral_constant<int, c + 3>{}, std::integral_constant<int, 0>{});
        if (i == 3) qpiece(std::integral_constant<int, c + 3>{}, std::integral_constant<int, 1>{});
        if (i == 5) qpiece(std::integral_constant<int, c + 3>{}, std::integral_constant<int, 2>{});
        if (i == 7) qpiece(std::integral_constant<int, c + 3>{}, std::integral_constant<int, 3>{});
        SB();
      }
#undef ARD
      if ((c & 1) == 0) {
        if ((c >> 1) + 3 < 8) eissue(u, (c >> 1) + 3);
        else if (park) eissue(0, (c >> 1) + 3 - 8);
      }
    });
#pragma unroll
    for (int a = 0; a < 2; ++a)
#pragma unroll
      for (int s = 0; s < 4; ++s) qf[a][s] = pack44(aq[a][2 * s] * CSC, aq[a][2 * s + 1] * CSC);
    const bf16x8 e0 = pack44(ae[0][0] * CSC, ae[0][1] * CSC), e1 = pack44(ae[1][0] * CSC, ae[1][1] * CSC);
    if (park) {
#pragma unroll
      for (int a = 0; a < 2; ++a)
#pragma unroll
        for (int s = 0; s < 4; ++s) *(bf16x8*)(qpark + w * 8192 + 1024 * (4 * a + s) + 16 * ln) = qf[a][s];
      *(bf16x8*)(qpark + 32768 + 256 * r16 + 16 * (4 * w + gq)) = e0;
      *(bf16x8*)(qpark + 32768 + 256 * (16 + r16) + 16 * (4 * w + gq)) = e1;
    } else {
      *(__attribute__((address_space(3))) bf16x8*)(LW + XCH + 256 * r16 + 16 * ((4 * w + gq) ^ r16)) = e0;
      *(__attribute__((address_space(3))) bf16x8*)(LW + XCH + 256 * (16 + r16) + 16 * ((4 * w + gq) ^ r16)) = e1;
      LDS_BAR();
#pragma unroll
      for (int s = 0; s < 4; ++s) qe[s] = ldl(L + XCH + 256 * (16 * eg + r16) + 16 * ((4 * s + gq) ^ r16));
    }
    STAMP(6);
  };

  auto attention = [&](auto U_, const int nk) __attribute__((always_inline)) {
    constexpr int u = decltype(U_)::value;
    f32x4 o[2][8] = {};
    f32x4 oe[8] = {};
    float l0 = 0.f, l1 = 0.f, le = 0.f;
    f32x4 negm[2] = {};
    f32x4 negme = {};
    const __amdgpu_buffer_rsrc_t rsK = __builtin_amdgcn_make_buffer_rsrc((void*)(u ? Kp1 : Kp0), (short)0, SK * DH * 2, 0x00020000);
    const __amdgpu_buffer_rsrc_t rsV = __builtin_amdgcn_make_buffer_rsrc((void*)(u ? Vp1 : Vp0), (short)0, SK * DH * 2, 0x00020000);
    auto tile = [&](const int t, auto F_) __attribute__((always_inline)) {
      constexpr bool FAST = decltype(F_)::value;
      const unsigned so = (unsigned)t * 16384u;
      const unsigned fdst = lds0 + (unsigned)w * 1024 + ((unsigned)(t & 1) << 15);
      const unsigned vdst = lds0 + (unsigned)w * 1024 + 3 * SLOT - ((unsigned)(t & 1) << 15);
#define PIECE_V(j) do { if constexpr (FAST) bdma<4096 * (j), 16384 + 4096 * (j), true>(rsV, voffKV, so, vdst); else piece(j); } while (0)
#define PIECE_K(j) do { if constexpr (FAST) bdma<4096 * (j), 32768 + 4096 * (j), true>(rsK, voffKV, so, fdst); else piece(j); } while (0)
      STAMP(7);
      WAIT_BAR(8);
      STAMP(0);
      if constexpr (!FAST) {
        if (u == 0 && t == 14 && !tb) {
          eissue(1, 0);
          eissue(1, 1);
          eissue(1, 2);
        }
        prep(nk + 2 * t + 3);
      }
      const float thr = t == 0 ? -__builtin_inff() : THR_L2;
      const float lo = t == 0 ? -__builtin_inff() : 0.f;
      f32x4 s[2][4], se[2];
      u32x4 pw[2][2], pwe;
      float m0, m1, me;
#define EX(x) __builtin_amdgcn_exp2f(x)
#define P2(S, i, LL, DST) do { const float a_ = EX(S[i]), b_ = EX(S[(i) + 1]); LL += a_; LL += b_; unsigned d_ = cvtpk(a_, b_); PIN(d_); PIN(LL); DST = d_; } while (0)
      const lds_cptr ka = (lds_cptr)(uintptr_t)kA_, kb = (lds_cptr)(uintptr_t)kB_;
      bf16x8 kf[16];
#define KRD(i) kf[i] = ldl((((i) >> 3) ? kb : ka) + 4096 * (((i) >> 2) & 1) + 512 * ((i) & 3))
      KRD(0); KRD(1); KRD(2);
      SB();
#pragma unroll
      for (int i = 0; i < 8; ++i) {
        const int kt = i >> 2, ks = i & 3;
        KRD(i + 3);
        if (ks == 0) {
          s[0][kt] = mfma16_head(kf[i], qf[0][0], negm[0]);
          s[1][kt] = mfma16_head(kf[i], qf[1][0], negm[1]);
          se[kt] = mfma16_head(kf[i], qe[0], negme);
        } else {
          s[0][kt] = MFMA16(kf[i], qf[0][ks], s[0][kt]);
          s[1][kt] = MFMA16(kf[i], qf[1][ks], s[1][kt]);
          se[kt] = MFMA16(kf[i], qe[ks], se[kt]);
        }
        if (i == 1) PIECE_V(0);
        if (i == 5) PIECE_V(1);
        if (i == 5) { m0 = MX3(s[0][0][0], s[0][0][1], s[0][0][2]); m1 = MX3(s[1][0][0], s[1][0][1], s[1][0][2]); PIN(m0); PIN(m1); }
        if (i == 6) { me = MX3(se[0][0], se[0][1], se[0][2]); m0 = MX3(m0, s[0][0][3], m0); PIN(me); PIN(m0); }
        if (i == 7) { m1 = MX3(m1, s[1][0][3], m1); me = MX3(me, se[0][3], me); PIN(m1); PIN(me); }
        SB();
      }
      m0 = MX3(m0, s[0][1][0], s[0][1][1]); m0 = MX3(m0, s[0][1][2], s[0][1][3]);
      m1 = MX3(m1, s[1][1][0], s[1][1][1]); m1 = MX3(m1, s[1][1][2], s[1][1][3]);
      me = MX3(me, se[1][0], se[1][1]); me = MX3(me, se[1][2], se[1][3]);
      if (__builtin_expect(__any(MX3(m0, m1, me) > thr), 0)) {
        const float d0 = fmaxf(rowmax4(m0), lo), d1 = fmaxf(rowmax4(m1), lo), de = fmaxf(rowmax4(me), lo);
        const float a0 = EX(-fmaxf(d0, 0.f)), a1 = EX(-fmaxf(d1, 0.f)), ae_ = EX(-fmaxf(de, 0.f));
        l0 *= a0; l1 *= a1; le *= ae_;
#pragma unroll
        for (int d = 0; d < 8; ++d) { o[0][d] *= a0; o[1][d] *= a1; oe[d] *= ae_; }
        s[0][0] -= d0; s[0][1] -= d0; s[1][0] -= d1; s[1][1] -= d1; se[0] -= de; se[1] -= de;
        negm[0] -= d0; negm[1] -= d1; negme -= de;
        PIN(negm[0]); PIN(negm[1]); PIN(negme);
        asm volatile("s_nop 1");
      }
      SB();
#pragma unroll
      for (int i = 0; i < 8; ++i) {
        const int kt = 2 + (i >> 2), ks = i & 3;
        if (i + 11 < 16) { KRD(i + 11); }
        if (ks == 0) {
          s[0][kt] = mfma16_head(kf[8 + i], qf[0][0], negm[0]);
          s[1][kt] = mfma16_head(kf[8 + i], qf[1][0], negm[1]);
        } else {
          s[0][kt] = MFMA16(kf[8 + i], qf[0][ks], s[0][kt]);
          s[1][kt] = MFMA16(kf[8 + i], qf[1][ks], s[1][kt]);
        }
        if (i < 4) P2(s[0][i >> 1], 2 * (i & 1), l0, pw[0][0][i]);
        else P2(s[1][(i - 4) >> 1], 2 * (i & 1), l1, pw[1][0][i - 4]);
        if (i & 1) P2(se[i >> 2], 2 * ((i >> 1) & 1), le, pwe[i >> 1]);
        if (i == 2) PIECE_V(2);
        if (i == 6) PIECE_V(3);
        SB();
      }
#undef KRD
      m0 = MX3(s[0][2][0], s[0][2][1], s[0][2][2]); m0 = MX3(m0, s[0][2][3], s[0][3][0]); m0 = MX3(m0, s[0][3][1], s[0][3][2]);
      m1 = MX3(s[1][2][0], s[1][2][1], s[1][2][2]); m1 = MX3(m1, s[1][2][3], s[1][3][0]); m1 = MX3(m1, s[1][3][1], s[1][3][2]);
      m0 = MX3(m0, s[0][3][3], m0); m1 = MX3(m1, s[1][3][3], m1);
      if (__builtin_expect(__any(MX3(m0, m1, m1) > THR_L2), 0)) {
        const float d0 = fmaxf(rowmax4(m0), 0.f), d1 = fmaxf(rowmax4(m1), 0.f);
        const float a0 = EX(-d0), a1 = EX(-d1);
        l0 *= a0; l1 *= a1;
#pragma unroll
        for (int d = 0; d < 8; ++d) { o[0][d] *= a0; o[1][d] *= a1; }
#pragma unroll
        for (int i = 0; i < 4; ++i) {
          const unsigned v0 = pw[0][0][i], v1 = pw[1][0][i];
          pw[0][0][i] = cvtpk(__uint_as_float(v0 << 16) * a0, __uint_as_float(v0 & 0xffff0000u) * a0);
          pw[1][0][i] = cvtpk(__uint_as_float(v1 << 16) * a1, __uint_as_float(v1 & 0xffff0000u) * a1);
        }
        s[0][2] -= d0; s[0][3] -= d0; s[1][2] -= d1; s[1][3] -= d1;
        negm[0] -= d0; negm[1] -= d1;
      }
      STAMP(2);
      WAIT_BAR(8);
      STAMP(3);
      if constexpr (!FAST) prep(nk + 2 * t + 4);
      {
        const lds_cptr a0 = (lds_cptr)(uintptr_t)vA0, a1 = (lds_cptr)(uintptr_t)vA1, b0 = (lds_cptr)(uintptr_t)vB0, b1 = (lds_cptr)(uintptr_t)vB1;
        bf16x8 vf[16];
#define VB(i) (((i) >> 3) ? (((i) & 1) ? b1 : b0) : (((i) & 1) ? a1 : a0))
#define VRD(i) vf[i] = tr2(VB(i) + 512 * (((i) & 7) >> 1), VB(i) + 512 * (((i) & 7) >> 1) + 4096)
        VRD(0); VRD(1);
        SB();
#pragma unroll
        for (int i = 0; i < 8; ++i) {
          VRD(i + 2);
          o[0][i] = MFMA16(vf[i], __builtin_bit_cast(bf16x8, pw[0][0]), o[0][i]);
          o[1][i] = MFMA16(vf[i], __builtin_bit_cast(bf16x8, pw[1][0]), o[1][i]);
          oe[i] = MFMA16(vf[i], __builtin_bit_cast(bf16x8, pwe), oe[i]);
          if (i < 4) P2(s[0][2 + (i >> 1)], 2 * (i & 1), l0, pw[0][1][i]);
          else P2(s[1][2 + ((i - 4) >> 1)], 2 * (i & 1), l1, pw[1][1][i - 4]);
          if (i == 1) PIECE_K(0);
          if (i == 5) PIECE_K(1);
          SB();
        }
#pragma unroll
        for (int i = 0; i < 8; ++i) {
          if (i + 10 < 16) { VRD(i + 10); }
          o[0][i] = MFMA16(vf[8 + i], __builtin_bit_cast(bf16x8, pw[0][1]), o[0][i]);
          o[1][i] = MFMA16(vf[8 + i], __builtin_bit_cast(bf16x8, pw[1][1]), o[1][i]);
          if (i == 1) PIECE_K(2);
          if (i == 5) PIECE_K(3);
          SB();
        }
#undef VB
#undef VRD
      }
#undef EX
#undef P2
      STAMP(5);
      const unsigned dir = (t & 1) ? 0u - 2 * SLOT : 2 * SLOT;
      kA_ += dir; kB_ += dir; vA0 += dir; vA1 += dir; vB0 += dir; vB1 += dir;
      PIN(kA_); PIN(kB_); PIN(vA0); PIN(vA1); PIN(vB0); PIN(vB1);
#undef PIECE_V
#undef PIECE_K
    };
#pragma unroll 1
    for (int t = 0; t < 14; ++t) tile(t, std::true_type{});
#pragma unroll 1
    for (int t = 14; t < 16; ++t) tile(t, std::false_type{});

    char* ob = (char*)o_ws + (size_t)((u * NB + b) * NH + h) * (40 * 1024);
    const int ln = lane_id();
    int w = w_;
    asm volatile("" : "+s"(w));
    const int eg = w & 1, kh = w >> 1;
    {
      const float i0 = 1.0f / rowsum4(l0), i1 = 1.0f / rowsum4(l1);
      char* of = ob + w * 8192 + 16 * ln;
#pragma unroll
      for (int uu = 0; uu < 4; ++uu) {
        *(bf16x8*)(of + 1024 * uu) = pack44(o[0][2 * uu] * i0, o[0][2 * uu + 1] * i0);
        *(bf16x8*)(of + 4096 + 1024 * uu) = pack44(o[1][2 * uu] * i1, o[1][2 * uu + 1] * i1);
      }
    }
    {
      constexpr unsigned EX0 = 3 * SLOT;
      const int r16 = ln & 15;
      const float me = -negme[0];
      le = rowsum4(le);
      LDS_BAR();
      if (ln < 16) {
        f32x2 ml = {me, le};
        *(__attribute__((address_space(3))) f32x2*)(LW + EX0 + 4096 * (w ^ 2) + 8 * ln) = ml;
      }
      LDS_BAR();
      f32x2 pml = *(const __attribute__((address_space(3))) f32x2*)(L + EX0 + 4096 * w + 8 * r16);
      const float M = fmaxf(me, pml[0]);
      const float sc = __builtin_amdgcn_exp2f(me - M), scp = __builtin_amdgcn_exp2f(pml[0] - M);
      const float f = sc / (le * sc + pml[1] * scp);
      asm volatile("s_waitcnt lgkmcnt(0)" ::: "memory");
      f32x4 keep[4];
#pragma unroll
      for (int i = 0; i < 4; ++i) {
        f32x4 snd;
#pragma unroll
        for (int r = 0; r < 4; ++r) {
          snd[r] = (kh ? oe[i][r] : oe[4 + i][r]) * f;
          keep[i][r] = (kh ? oe[4 + i][r] : oe[i][r]) * f;
        }
        *(__attribute__((address_space(3))) f32x4*)(LW + EX0 + 4096 * w + 1024 * i + 16 * ln) = snd;
      }
      LDS_BAR();
#pragma unroll
      for (int i = 0; i < 4; ++i) keep[i] += *(const __attribute__((address_space(3))) f32x4*)(L + EX0 + 4096 * (w ^ 2) + 1024 * i + 16 * ln);
      char* ef = ob + 32768 + 4096 * eg + 2048 * kh + 16 * ln;
      *(bf16x8*)(ef) = pack44(keep[0], keep[1]);
      *(bf16x8*)(ef + 1024) = pack44(keep[2], keep[3]);
    }
  };

#pragma unroll 1
  for (int i = 0; i < (tb ? 2 : 1); ++i) qphase((tb && i == 0) ? 1 : 0, 16 * i, tb && i == 0);
  attention(std::integral_constant<int, 0>{}, tb ? 32 : 16);
  __builtin_amdgcn_s_setprio(0);
  if (tb) {
    const int ln = lane_id();
#pragma unroll
    for (int a = 0; a < 2; ++a)
#pragma unroll
      for (int s = 0; s < 4; ++s) qf[a][s] = *(const bf16x8*)(qpark + w * 8192 + 1024 * (4 * a + s) + 16 * ln);
#pragma unroll
    for (int s = 0; s < 4; ++s) qe[s] = *(const bf16x8*)(qpark + 32768 + 256 * (16 * eg + (ln & 15)) + 16 * (4 * s + (ln >> 4)));
#pragma unroll
    for (int a = 0; a < 2; ++a)
#pragma unroll
      for (int s = 0; s < 4; ++s) PIN(qf[a][s]);
#pragma unroll
    for (int s = 0; s < 4; ++s) PIN(qe[s]);
  } else {
    qphase(1, 48, false);
  }
  attention(std::integral_constant<int, 1>{}, 64);
  asm volatile("s_waitcnt vmcnt(0)" ::: "memory");
}

constexpr unsigned S2 = 36864, NS2 = 4, LDS2 = S2 * NS2;

__global__ __launch_bounds__(768, 3) void oproj_kernel(char* __restrict__ ws, const u16* __restrict__ woA, const u16* __restrict__ woB,
                                                       u16* __restrict__ out) {
  extern __shared__ __attribute__((aligned(16))) char smem[];
  const int tid = threadIdx.x, lane = tid & 63;
  const int w = __builtin_amdgcn_readfirstlane(tid >> 6);
  const int wn = w & 3, wk = w >> 2;
  const int g = blockIdx.x, xcd = g & 7, idx = g >> 3;
  const int blkk = xcd >> 2, mt = 4 * (xcd & 3) + (idx >> 3), nt = (idx >> 1) & 3, kh = idx & 1;
  const int tile = (blkk * 16 + mt) * 4 + nt;
  const unsigned lds0 = (unsigned)(uintptr_t)smem;
  const lds_cptr L = (lds_cptr)smem;
  const lds_ptr LW = (lds_ptr)smem;
  const int r16 = lane & 15, gq = lane >> 4, q4 = (lane & 15) >> 2, p4 = lane & 3;
  if (w >= 8) {
    const char* so_ = (const char*)ws + (size_t)((blkk * NB + mt) * NH + kh * 16) * (40 * 1024) + 4096 * (wn >> 1) + 1024 * (wn & 1);
    const char* sw_ = (const char*)((blkk ? woB : woA) + (size_t)(kh * 2048) * DM + nt * 128);
    const __amdgpu_buffer_rsrc_t rso = __builtin_amdgcn_make_buffer_rsrc((void*)so_, (short)0, 4 << 20, 0x00020000);
    const __amdgpu_buffer_rsrc_t rsw = __builtin_amdgcn_make_buffer_rsrc((void*)sw_, (short)0, 4 << 20, 0x00020000);
    const unsigned vo = 16 * lane;
    const unsigned vw = (unsigned)(8 * (wn >> 1) + ((lane >> 2) & 7)) * (DM * 2) + 128 * (wn & 1) + 64 * (lane >> 5) + 16 * ((lane & 3) ^ (2 * ((lane >> 4) & 1)));
    auto chunk = [&](int c) {
      const unsigned cc = (unsigned)c & 31u;
      const unsigned oo = (cc >> 1) * 40960u + (cc & 1) * 2048u, ow = cc * (64u * DM * 2);
      const unsigned dst = lds0 + (unsigned)wn * 1024 + (unsigned)(c & 3) * S2;
      bdma<0, 0, false>(rso, vo, oo, dst);
      bdma<20480, 0, false>(rsw, vw, ow, dst);
      bdma<4096, 8192, false>(rso, vo, oo, dst);
      bdma<20480 + 4096, 16 * DM * 2, false>(rsw, vw, ow, dst);
      bdma<8192, 16384, false>(rso, vo, oo, dst);
      bdma<20480 + 8192, 32 * DM * 2, false>(rsw, vw, ow, dst);
      bdma<12288, 24576, false>(rso, vo, oo, dst);
      bdma<20480 + 12288, 48 * DM * 2, false>(rsw, vw, ow, dst);
      bdma<16384, 32768, false>(rso, vo, oo, dst);
    };
    chunk(0);
    chunk(1);
    chunk(2);
#pragma unroll 1
    for (int c = 0; c < 32; ++c) {
      WAIT_BAR(18);
      chunk(c + 3);
    }
    WAIT_BAR(0);
    return;
  }
  const unsigned ob0 = 1024 * wk + 16 * lane;
  const unsigned wb0 = 20480 + 2048 * (4 * wk + (gq >> 1)) + 512 * wn + 64 * (4 * (gq & 1) + q4) + 32 * (0 ^ (gq & 1)) + 8 * p4;
  const unsigned wb1 = 20480 + 2048 * (4 * wk + (gq >> 1)) + 512 * wn + 64 * (4 * (gq & 1) + q4) + 32 * (1 ^ (gq & 1)) + 8 * p4;
#define DUMP()
  f32x4 acc[2][10] = {};
  STAMP(0);
#pragma unroll 1
  for (int c = 0; c < 32; ++c) {
    LDS_BAR();
    const lds_cptr sl = L + (c & 3) * S2;
    bf16x8 fa0 = tr2(sl + wb0, sl + wb0 + 4096), fa1 = tr2(sl + wb1, sl + wb1 + 4096);
    bf16x8 fb[10];
    fb[0] = ldl(sl + ob0);
    fb[1] = ldl(sl + ob0 + 2048);
    fb[2] = ldl(sl + ob0 + 4096);
    SB();
#pragma unroll
    for (int k = 0; k < 10; ++k) {
      if (k + 3 < 10) fb[k + 3] = ldl(sl + ob0 + 2048 * (k + 3));
      acc[0][k] = MFMA16(fa0, fb[k], acc[0][k]);
      acc[1][k] = MFMA16(fa1, fb[k], acc[1][k]);
      SB();
    }
  }
  STAMP(1);
  WAIT_BAR(0);
  f32x4 res[10];
  auto red = [&](auto K_) __attribute__((always_inline)) {
    constexpr int kk = decltype(K_)::value;
#pragma unroll
    for (int i = 0; i < 10; ++i) *(__attribute__((address_space(3))) f32x4*)(LW + 10240 * w + 1024 * i + 16 * lane) = acc[1 - kk][i];
    LDS_BAR();
#pragma unroll
    for (int i = 0; i < 10; ++i) res[i] = acc[kk][i] + *(const __attribute__((address_space(3))) f32x4*)(L + 10240 * (w ^ 4) + 1024 * i + 16 * lane);
  };
  if (wk) red(std::integral_constant<int, 1>{});
  else red(std::integral_constant<int, 0>{});
  STAMP(2);
  unsigned* sync = (unsigned*)(ws + WS_SYNC + (size_t)tile * 128);
  volatile __attribute__((address_space(3))) unsigned* tk = (volatile __attribute__((address_space(3))) unsigned*)(LW + 90112);
  const __amdgpu_buffer_rsrc_t rs = __builtin_amdgcn_make_buffer_rsrc(ws + WS_PART + (size_t)tile * 163840, 0, 163840, 0x00020000);
  const unsigned poff = (unsigned)w * 10240 + 16 * lane;
  const bool mine = (wn >> 1) == kh;
  auto publish = [&]() {
#pragma unroll
    for (int i = 0; i < 10; ++i) __builtin_amdgcn_raw_buffer_store_b128(__builtin_bit_cast(u32x4, res[i]), rs, 81920u * kh + poff + 1024 * i, 0, 16);
  };
  if (!mine) publish();
  asm volatile("s_waitcnt vmcnt(0)" ::: "memory");
  __builtin_amdgcn_s_barrier();
  if (tid == 0) {
    __hip_atomic_store(sync + kh, 1u, __ATOMIC_RELAXED, __HIP_MEMORY_SCOPE_AGENT);
    unsigned st = 0u, spins = 0;
    while (__hip_atomic_load(sync + (kh ^ 1), __ATOMIC_RELAXED, __HIP_MEMORY_SCOPE_AGENT) == 0u) {
      __builtin_amdgcn_s_sleep(1);
      if (++spins == SPIN_MAX) { st = 1u; break; }
    }
    *tk = st;
  }
  LDS_BAR();
  unsigned st = __builtin_amdgcn_readfirstlane(*tk);
  if (st == 1u) {
    if (mine) publish();
    asm volatile("s_waitcnt vmcnt(0)" ::: "memory");
    __builtin_amdgcn_s_barrier();
    if (tid == 0) {
      unsigned e1 = 0u;
      __hip_atomic_compare_exchange_strong(sync + 16 + (kh ^ 1), &e1, 2u, __ATOMIC_RELAXED, __ATOMIC_RELAXED, __HIP_MEMORY_SCOPE_AGENT);
      bool gave = false;
      if (e1 != 1u) {
        unsigned e2 = 0u;
        gave = __hip_atomic_compare_exchange_strong(sync + 16 + kh, &e2, 1u, __ATOMIC_RELAXED, __ATOMIC_RELAXED, __HIP_MEMORY_SCOPE_AGENT);
      }
      if (!gave)
        while (__hip_atomic_load(sync + (kh ^ 1), __ATOMIC_RELAXED, __HIP_MEMORY_SCOPE_AGENT) == 0u) __builtin_amdgcn_s_sleep(1);
      *tk = gave ? 1u : 0u;
    }
    LDS_BAR();
    st = __builtin_amdgcn_readfirstlane(*tk);
    if (st == 1u) return;
  }
  __builtin_amdgcn_fence(__ATOMIC_ACQUIRE, "wavefront");
  STAMP(4);
  auto finish = [&]() {
#pragma unroll
    for (int i = 0; i < 10; ++i) {
      u16* orow = out + (size_t)(mt * 160 + 16 * i + r16) * 1024 + blkk * 512 + nt * 128 + 32 * wn + 16 * wk + 4 * gq;
      const f32x4 v = __builtin_bit_cast(f32x4, __builtin_amdgcn_raw_buffer_load_b128(rs, 81920u * (kh ^ 1) + poff + 1024 * i, 0, 16));
      u32x2 pv = {cvtpk(res[i][0] + v[0], res[i][1] + v[1]), cvtpk(res[i][2] + v[2], res[i][3] + v[3])};
      *(u32x2*)orow = pv;
    }
  };
  if (mine) finish();
  if (tid == 64 * (2 * (kh ^ 1))) {
    unsigned expect = 0u;
    __hip_atomic_compare_exchange_strong(sync + 16 + (kh ^ 1), &expect, 2u, __ATOMIC_RELAXED, __ATOMIC_RELAXED, __HIP_MEMORY_SCOPE_AGENT);
    tk[1] = expect;
  }
  LDS_BAR();
  if (!mine && __builtin_amdgcn_readfirstlane(tk[1]) == 1u) {
    __builtin_amdgcn_fence(__ATOMIC_ACQUIRE, "wavefront");
    finish();
  }
  STAMP(5);
  DUMP();
}

extern "C" void kernel_launch(void* const* d_in, const int* in_sizes, int n_in, void* d_out, int out_size, void* d_ws, size_t ws_size,
                              hipStream_t stream) {
  const u16* xA = (const u16*)d_in[0];
  const u16* kA = (const u16*)d_in[1];
  const u16* vA = (const u16*)d_in[2];
  const u16* wqA = (const u16*)d_in[3];
  const u16* woA = (const u16*)d_in[4];
  const u16* xB = (const u16*)d_in[5];
  const u16* kB = (const u16*)d_in[6];
  const u16* vB = (const u16*)d_in[7];
  const u16* wqB = (const u16*)d_in[8];
  const u16* woB = (const u16*)d_in[9];
  u16* o_ws = (u16*)d_ws;
  (void)hipFuncSetAttribute((const void*)attn_kernel, hipFuncAttributeMaxDynamicSharedMemorySize, (int)LDS1);
  (void)hipFuncSetAttribute((const void*)oproj_kernel, hipFuncAttributeMaxDynamicSharedMemorySize, (int)LDS2);
  hipLaunchKernelGGL(attn_kernel, dim3(512), dim3(256), LDS1, stream, xA, kA, vA, wqA, xB, kB, vB, wqB, o_ws);
  hipLaunchKernelGGL(oproj_kernel, dim3(256), dim3(768), LDS2, stream, (char*)d_ws, woA, woB, (u16*)d_out);
}
```

```cpp
#include <hip/hip_runtime.h>
#include <hip/hip_bf16.h>
#include <stdint.h>
#include <type_traits>
#include <utility>

typedef short bf16x8 __attribute__((ext_vector_type(8)));
typedef short s16x4 __attribute__((ext_vector_type(4)));
typedef float f32x4 __attribute__((ext_vector_type(4)));
typedef float f32x2 __attribute__((ext_vector_type(2)));
typedef __bf16 bf16x2_t __attribute__((ext_vector_type(2)));
typedef unsigned u32x4 __attribute__((ext_vector_type(4)));
typedef unsigned u32x2 __attribute__((ext_vector_type(2)));
typedef unsigned short u16;
typedef __attribute__((address_space(3))) char* lds_ptr;
typedef __attribute__((address_space(3))) const char* lds_cptr;
typedef short v4i16_t __attribute__((ext_vector_type(4)));

#define STAMP(i)
#define DI __device__ __forceinline__
#define MFMA16(a, b, c) __builtin_amdgcn_mfma_f32_16x16x32_bf16((a), (b), (c), 0, 0, 0)
#define STR2(x) #x
#define STR(x) STR2(x)
#define WAIT_BAR(N) asm volatile("s_waitcnt vmcnt(" STR(N) ") lgkmcnt(0)\n\ts_barrier" ::: "memory")
#define LDS_BAR() asm volatile("s_waitcnt lgkmcnt(0)\n\ts_barrier" ::: "memory")
#define SB() __builtin_amdgcn_sched_barrier(0)
#define MX3(a, b, c) __builtin_fmaxf(__builtin_fmaxf((a), (b)), (c))
#define PIN(x) asm volatile("" : "+v"(x))

constexpr int SQ = 160, SK = 1024, DH = 128, NH = 32, NB = 16, DM = 512, HD = 4096;
constexpr float CSC = 0.088388347648318447f * 1.4426950408889634f;
constexpr float THR_L2 = 8.0f;

template <int N, class F, int... I>
DI void static_for_impl(F&& f, std::integer_sequence<int, I...>) { (f(std::integral_constant<int, I>{}), ...); }
template <int N, class F>
DI void static_for(F&& f) { static_for_impl<N>(f, std::make_integer_sequence<int, N>{}); }

DI unsigned cvtpk(float lo, float hi) {
  f32x2 v = {lo, hi};
  bf16x2_t b = __builtin_convertvector(v, bf16x2_t);
  return __builtin_bit_cast(unsigned, b);
}
DI bf16x8 pack44(const f32x4& a, const f32x4& b) {
  u32x4 w = {cvtpk(a[0], a[1]), cvtpk(a[2], a[3]), cvtpk(b[0], b[1]), cvtpk(b[2], b[3])};
  return __builtin_bit_cast(bf16x8, w);
}
DI s16x4 trd(lds_cptr p) {
  return __builtin_bit_cast(s16x4, __builtin_amdgcn_ds_read_tr16_b64_v4i16((__attribute__((address_space(3))) v4i16_t*)p));
}
DI bf16x8 tr2(lds_cptr p0, lds_cptr p1) {
  s16x4 a = trd(p0), b = trd(p1);
  return __builtin_shufflevector(a, b, 0, 1, 2, 3, 4, 5, 6, 7);
}
DI bf16x8 ldl(lds_cptr p) { return *(const __attribute__((address_space(3))) bf16x8*)p; }
DI int lane_id() {
  int l;
  asm volatile("v_mbcnt_lo_u32_b32 %0, -1, 0\n\tv_mbcnt_hi_u32_b32 %0, -1, %0" : "=v"(l));
  return l;
}
DI void dma16(const char* sbase, unsigned voff, unsigned lds_dst) {
  asm volatile("s_mov_b32 m0, %2\n\ts_nop 0\n\tglobal_load_lds_dwordx4 %1, %0" ::"s"(sbase), "v"(voff), "s"(lds_dst) : "memory");
}
DI void dma16s(const char* sbase, unsigned voff, unsigned lds_dst) {
  asm volatile("s_mov_b32 m0, %2\n\ts_nop 0\n\tglobal_load_lds_dwordx4 %1, %0 nt" ::"s"(sbase), "v"(voff), "s"(lds_dst) : "memory");
}
template <int IM, int IMG, bool NT>
DI void bdma(__amdgpu_buffer_rsrc_t rs, unsigned voff, unsigned so, unsigned dst) {
  unsigned tmp;
  if constexpr (NT)
    asm volatile("s_add_u32 m0, %[dst], %[im]\n\ts_add_u32 %[tmp], %[so], %[ig]\n\tbuffer_load_dwordx4 %[v], %[rs], %[tmp] offen nt lds"
                 : [tmp] "=&s"(tmp)
                 : [dst] "s"(dst), [so] "s"(so), [im] "i"(IM), [ig] "i"(IMG), [v] "v"(voff), [rs] "s"(rs)
                 : "memory", "scc");
  else
    asm volatile("s_add_u32 m0, %[dst], %[im]\n\ts_add_u32 %[tmp], %[so], %[ig]\n\tbuffer_load_dwordx4 %[v], %[rs], %[tmp] offen lds"
                 : [tmp] "=&s"(tmp)
                 : [dst] "s"(dst), [so] "s"(so), [im] "i"(IM), [ig] "i"(IMG), [v] "v"(voff), [rs] "s"(rs)
                 : "memory", "scc");
}
DI float rowmax4(float x) {
  auto r = __builtin_amdgcn_permlane16_swap(__float_as_uint(x), __float_as_uint(x), false, false);
  x = fmaxf(__uint_as_float(r[0]), __uint_as_float(r[1]));
  auto s = __builtin_amdgcn_permlane32_swap(__float_as_uint(x), __float_as_uint(x), false, false);
  return fmaxf(__uint_as_float(s[0]), __uint_as_float(s[1]));
}
DI float rowsum4(float x) {
  auto r = __builtin_amdgcn_permlane16_swap(__float_as_uint(x), __float_as_uint(x), false, false);
  x = __uint_as_float(r[0]) + __uint_as_float(r[1]);
  auto s = __builtin_amdgcn_permlane32_swap(__float_as_uint(x), __float_as_uint(x), false, false);
  return __uint_as_float(s[0]) + __uint_as_float(s[1]);
}
DI f32x4 mfma16_head(bf16x8 a, bf16x8 b, const f32x4& c) {
  f32x4 d;
  asm volatile("v_mfma_f32_16x16x32_bf16 %0, %1, %2, %3" : "=&v"(d) : "v"(a), "v"(b), "v"(c));
  return d;
}

constexpr unsigned SLOT = 16384, XCH = 65536, LDS1 = 81920;
constexpr size_t WS_SYNC = (size_t)2 * NB * SQ * HD * 2;
constexpr size_t WS_PART = WS_SYNC + 128 * 128;
constexpr size_t WS_QPARK = (size_t)64 << 20;
#define SPIN_MAX 200000u

__global__ __launch_bounds__(256, 2) void attn_kernel(const u16* __restrict__ xA, const u16* __restrict__ kA, const u16* __restrict__ vA,
                                                      const u16* __restrict__ wqA, const u16* __restrict__ xB, const u16* __restrict__ kB,
                                                      const u16* __restrict__ vB, const u16* __restrict__ wqB, u16* __restrict__ o_ws,
                                                      const u16* __restrict__ woA, const u16* __restrict__ woB) {
  extern __shared__ __attribute__((aligned(16))) char smem[];
  const int tid = threadIdx.x, lane = tid & 63;
  const int w = __builtin_amdgcn_readfirstlane(tid >> 6);
  const int g = blockIdx.x, xcd = g & 7, idx = g >> 3;
  const int h = 4 * xcd + (idx & 3), b = idx >> 2;
  const unsigned lds0 = (unsigned)(uintptr_t)smem;
  const lds_cptr L = (lds_cptr)smem;
  const lds_ptr LW = (lds_ptr)smem;

  const size_t kvoff = (size_t)(b * NH + h) * SK * DH * 2;
  const char* Kp0 = (const char*)kA + kvoff;
  const char* Vp0 = (const char*)vA + kvoff;
  const char* Kp1 = (const char*)kB + kvoff;
  const char* Vp1 = (const char*)vB + kvoff;
  const char* Wp0 = (const char*)wqA + h * 256;
  const char* Wp1 = (const char*)wqB + h * 256;
  const char* Xp0 = (const char*)(xA + (size_t)b * SQ * DM);
  const char* Xp1 = (const char*)(xB + (size_t)b * SQ * DM);

  const unsigned rowl = 8 * (w >> 1) + ((lane >> 2) & 7);
  const unsigned chs = 8 * (w & 1) + 4 * (lane >> 5) + ((lane & 3) ^ (2 * (w >> 1) + ((lane >> 4) & 1)));
  const unsigned voffKV = rowl * 256 + chs * 16;
  auto mk_voffE = [&]() {
    const int ln = lane_id();
    return (unsigned)(8 * w + (ln >> 3)) * (DM * 2) + 16 * ((ln & 7) ^ (4 * (w & 1) + (ln >> 4)));
  };
  auto mk_voffX = [&]() {
    const int ln = lane_id();
    return (unsigned)(16 * w + (ln >> 2)) * (DM * 2) + 16 * ((ln & 3) ^ ((ln >> 4) & 3));
  };
  auto mk_voffW = [&]() {
    unsigned v = voffKV;
    asm volatile("" : "+v"(v));
    return ((v & ~255u) << 5) | (v & 255u);
  };

  const bool tb = g >= 256;
  const int w_ = w;
  if (tb) __builtin_amdgcn_s_setprio(1);

  const char *d_src, *d_src2;
  unsigned d_dst;
  bool d_kv;
  auto prep = [&](int nn) {
    d_dst = lds0 + (unsigned)(nn & 3) * SLOT + (unsigned)w * 1024;
    const int ph = nn >> 4, j = nn & 15;
    const bool isq = tb ? (ph < 2 || ph >= 6) : (ph == 0 || ph == 3 || ph >= 6);
    const int un = tb ? (ph == 0 || ph == 4 || ph == 5) : (ph >= 3 && ph < 6);
    const int hf = tb ? (ph & 1) : (ph == 2 || ph == 5);
    d_kv = !isq;
    if (isq) {
      d_src = (un ? Wp1 : Wp0) + (size_t)j * (32 * 8192);
      d_src2 = (un ? Xp1 : Xp0) + 64 * j;
    } else {
      d_src = ((j & 1) ? (un ? Vp1 : Vp0) : (un ? Kp1 : Kp0)) + (size_t)(8 * hf + (j >> 1)) * 16384;
      d_src2 = d_src;
    }
  };
  auto piece = [&](int j) {
    const unsigned dst = d_dst + 4096u * (unsigned)j;
    if (d_kv) dma16s(d_src + 4096 * j, voffKV, dst);
    else if (j < 2) dma16(d_src + (size_t)j * (16 * 8192), mk_voffW(), dst);
    else dma16(d_src2 + (size_t)(j - 2) * (64 * 1024), mk_voffX(), dst);
  };
  auto issue = [&](int nn) {
    prep(nn);
    piece(0);
    piece(1);
    piece(2);
    piece(3);
  };
  auto eissue = [&](int u, int e) { dma16((u ? Xp1 : Xp0) + 128 * 1024 + 128 * e, mk_voffE(), lds0 + XCH + 4096u * (unsigned)(e & 3) + (unsigned)w * 1024); };

  const int eg = w & 1, kh = w >> 1;
  unsigned kA_, kB_, vA0, vA1, vB0, vB1;
  {
    const int r16 = lane & 15, gq = lane >> 4, q4 = (lane & 15) >> 2, p4 = lane & 3;
    const int pa = (0x78 >> (2 * (r16 >> 2))) & 3, pg = (0x78 >> (2 * gq)) & 3;
    const unsigned kb = lds0 + 2048 * (pa >> 1) + 64 * (4 * (pa & 1) + (r16 & 3)) + 16 * (gq ^ pa);
    kA_ = kb + 8192 * kh;
    kB_ = kb + 8192 * (1 - kh);
    const unsigned v0 = lds0 + SLOT + 2048 * (pg >> 1) + 64 * (4 * (pg & 1) + q4) + 16 * ((0 + (p4 >> 1)) ^ pg) + 8 * (p4 & 1);
    const unsigned v1 = lds0 + SLOT + 2048 * (pg >> 1) + 64 * (4 * (pg & 1) + q4) + 16 * ((2 + (p4 >> 1)) ^ pg) + 8 * (p4 & 1);
    vA0 = v0 + 8192 * kh;
    vA1 = v1 + 8192 * kh;
    vB0 = v0 + 8192 * (1 - kh);
    vB1 = v1 + 8192 * (1 - kh);
  }

  if (g == 0 && tid < 128) {
    unsigned* sy = (unsigned*)((char*)o_ws + WS_SYNC) + tid * 32;
    sy[0] = 0u;
    sy[1] = 0u;
    sy[16] = 0u;
    sy[17] = 0u;
  }
  {
    const int u0 = tb ? 1 : 0;
    eissue(u0, 0);
    eissue(u0, 1);
    eissue(u0, 2);
  }
  issue(0);
  issue(1);
  issue(2);
  {
    const size_t wo_ = (size_t)(8 * idx + (tid >> 6)) * 8192 + (size_t)xcd * 1024 + (tid & 63) * 16;
    u32x4 t0 = *(const u32x4*)((const char*)wqA + wo_), t1 = *(const u32x4*)((const char*)wqA + wo_ + 4 * 8192);
    u32x4 t2 = *(const u32x4*)((const char*)wqB + wo_), t3 = *(const u32x4*)((const char*)wqB + wo_ + 4 * 8192);
    const size_t xo_ = (size_t)g * 5120 + tid * 16;
    u32x4 t4 = *(const u32x4*)((const char*)xA + xo_), t5 = *(const u32x4*)((const char*)xB + xo_);
    u32x4 t6 = {}, t7 = {};
    if (tid < 64) { t6 = *(const u32x4*)((const char*)xA + xo_ + 4096); t7 = *(const u32x4*)((const char*)xB + xo_ + 4096); }
    asm volatile("" ::"v"(t0), "v"(t1), "v"(t2), "v"(t3), "v"(t4), "v"(t5), "v"(t6), "v"(t7));
  }

  bf16x8 qf[2][4];
  bf16x8 qe[4];
  char* qpark = (char*)o_ws + WS_QPARK + (size_t)(g & 255) * 40960;

  auto qphase = [&](const int u, const int n0, const bool park) __attribute__((always_inline)) {
    STAMP(1);
    const int ln = lane_id();
    const int r16 = ln & 15, gq = ln >> 4, q4 = (ln & 15) >> 2, p4 = ln & 3;
    const unsigned wb0 = 2048 * gq + 64 * (0 + q4) + 16 * (p4 ^ ((2 * gq + 0) & 3));
    const unsigned wb1 = 2048 * gq + 64 * (4 + q4) + 16 * (p4 ^ ((2 * gq + 1) & 3));
    const unsigned we0 = wb0 + 512 * w, we1 = wb1 + 512 * w;
    const unsigned xb = 8192 + 64 * (32 * w + r16) + 16 * (gq ^ ((r16 >> 2) & 3));
    const unsigned xe0 = XCH + 128 * r16 + 16 * ((0 + gq) ^ ((r16 >> 1) & 7));
    const unsigned xe1 = XCH + 128 * r16 + 16 * ((4 + gq) ^ ((r16 >> 1) & 7));
    f32x4 aq[2][8] = {};
    f32x4 ae[2][2] = {};
    const __amdgpu_buffer_rsrc_t rsW = __builtin_amdgcn_make_buffer_rsrc((void*)(u ? Wp1 : Wp0), (short)0, DM * HD * 2, 0x00020000);
    const __amdgpu_buffer_rsrc_t rsX = __builtin_amdgcn_make_buffer_rsrc((void*)(u ? Xp1 : Xp0), (short)0, SQ * DM * 2, 0x00020000);
    const unsigned vW = mk_voffW(), vX = mk_voffX();
    const unsigned qdst = lds0 + (unsigned)w * 1024;
    auto qpiece = [&](auto C_, auto J_) __attribute__((always_inline)) {
      constexpr int cn = decltype(C_)::value, j = decltype(J_)::value;
      if constexpr (cn < 16) {
        if constexpr (j < 2) bdma<(cn & 3) * SLOT + 4096 * j, cn * (32 * 8192) + j * (16 * 8192), false>(rsW, vW, 0u, qdst);
        else bdma<(cn & 3) * SLOT + 4096 * j, 64 * cn + (j - 2) * 65536, false>(rsX, vX, 0u, qdst);
      } else {
        piece(j);
      }
    };
    static_for<16>([&](auto C_) __attribute__((always_inline)) {
      constexpr int c = decltype(C_)::value;
      WAIT_BAR(8);
      const lds_cptr sl = L + (c & 3) * SLOT;
      const lds_cptr sxe = L + 4096 * ((c >> 1) & 3) + ((c & 1) ? xe1 : xe0);
      if constexpr (c + 3 >= 16) prep(n0 + c + 3);
      bf16x8 xm[2], xe[2], af[10];
#define ARD(i) af[i] = ((i) < 8) ? tr2(sl + wb0 + 512 * ((i) >> 1) + 8 * ((i) & 1), sl + wb1 + 512 * ((i) >> 1) + 8 * ((i) & 1)) \
                                 : tr2(sl + we0 + 8 * ((i) & 1), sl + we1 + 8 * ((i) & 1))
      xm[0] = ldl(sl + xb);
      xm[1] = ldl(sl + xb + 1024);
      ARD(0);
      ARD(1);
      SB();
#pragma unroll
      for (int i = 0; i < 10; ++i) {
        if (i + 2 < 10) { ARD(i + 2); }
        if (i == 4) xe[0] = ldl(sxe);
        if (i == 5) xe[1] = ldl(sxe + 2048);
        if (i < 8) {
          aq[0][i] = MFMA16(af[i], xm[0], aq[0][i]);
          aq[1][i] = MFMA16(af[i], xm[1], aq[1][i]);
        } else {
          ae[0][i - 8] = MFMA16(af[i], xe[0], ae[0][i - 8]);
          ae[1][i - 8] = MFMA16(af[i], xe[1], ae[1][i - 8]);
        }
        if (i == 1) qpiece(std::integral_constant<int, c + 3>{}, std::integral_constant<int, 0>{});
        if (i == 3) qpiece(std::integral_constant<int, c + 3>{}, std::integral_constant<int, 1>{});
        if (i == 5) qpiece(std::integral_constant<int, c + 3>{}, std::integral_constant<int, 2>{});
        if (i == 7) qpiece(std::integral_constant<int, c + 3>{}, std::integral_constant<int, 3>{});
        SB();
      }
#undef ARD
      if ((c & 1) == 0) {
        if ((c >> 1) + 3 < 8) eissue(u, (c >> 1) + 3);
        else if (park) eissue(0, (c >> 1) + 3 - 8);
      }
    });
#pragma unroll
    for (int a = 0; a < 2; ++a)
#pragma unroll
      for (int s = 0; s < 4; ++s) qf[a][s] = pack44(aq[a][2 * s] * CSC, aq[a][2 * s + 1] * CSC);
    const bf16x8 e0 = pack44(ae[0][0] * CSC, ae[0][1] * CSC), e1 = pack44(ae[1][0] * CSC, ae[1][1] * CSC);
    if (park) {
#pragma unroll
      for (int a = 0; a < 2; ++a)
#pragma unroll
        for (int s = 0; s < 4; ++s) *(bf16x8*)(qpark + w * 8192 + 1024 * (4 * a + s) + 16 * ln) = qf[a][s];
      *(bf16x8*)(qpark + 32768 + 256 * r16 + 16 * (4 * w + gq)) = e0;
      *(bf16x8*)(qpark + 32768 + 256 * (16 + r16) + 16 * (4 * w + gq)) = e1;
    } else {
      *(__attribute__((address_space(3))) bf16x8*)(LW + XCH + 256 * r16 + 16 * ((4 * w + gq) ^ r16)) = e0;
      *(__attribute__((address_space(3))) bf16x8*)(LW + XCH + 256 * (16 + r16) + 16 * ((4 * w + gq) ^ r16)) = e1;
      LDS_BAR();
#pragma unroll
      for (int s = 0; s < 4; ++s) qe[s] = ldl(L + XCH + 256 * (16 * eg + r16) + 16 * ((4 * s + gq) ^ r16));
    }
    STAMP(6);
  };

  auto attention = [&](auto U_, const int nk) __attribute__((always_inline)) {
    constexpr int u = decltype(U_)::value;
    f32x4 o[2][8] = {};
    f32x4 oe[8] = {};
    float l0 = 0.f, l1 = 0.f, le = 0.f;
    f32x4 negm[2] = {};
    f32x4 negme = {};
    const __amdgpu_buffer_rsrc_t rsK = __builtin_amdgcn_make_buffer_rsrc((void*)(u ? Kp1 : Kp0), (short)0, SK * DH * 2, 0x00020000);
    const __amdgpu_buffer_rsrc_t rsV = __builtin_amdgcn_make_buffer_rsrc((void*)(u ? Vp1 : Vp0), (short)0, SK * DH * 2, 0x00020000);
    auto tile = [&](const int t, auto F_) __attribute__((always_inline)) {
      constexpr bool FAST = decltype(F_)::value;
      const unsigned so = (unsigned)t * 16384u;
      const unsigned fdst = lds0 + (unsigned)w * 1024 + ((unsigned)(t & 1) << 15);
      const unsigned vdst = lds0 + (unsigned)w * 1024 + 3 * SLOT - ((unsigned)(t & 1) << 15);
#define PIECE_V(j) do { if constexpr (FAST) bdma<4096 * (j), 16384 + 4096 * (j), true>(rsV, voffKV, so, vdst); else piece(j); } while (0)
#define PIECE_K(j) do { if constexpr (FAST) bdma<4096 * (j), 32768 + 4096 * (j), true>(rsK, voffKV, so, fdst); else piece(j); } while (0)
      STAMP(7);
      WAIT_BAR(8);
      STAMP(0);
      if constexpr (!FAST) {
        if (u == 0 && t == 14 && !tb) {
          eissue(1, 0);
          eissue(1, 1);
          eissue(1, 2);
        }
        prep(nk + 2 * t + 3);
      }
      const float thr = t == 0 ? -__builtin_inff() : THR_L2;
      const float lo = t == 0 ? -__builtin_inff() : 0.f;
      f32x4 s[2][4], se[2];
      u32x4 pw[2][2], pwe;
      float m0, m1, me;
#define EX(x) __builtin_amdgcn_exp2f(x)
#define P2(S, i, LL, DST) do { const float a_ = EX(S[i]), b_ = EX(S[(i) + 1]); LL += a_; LL += b_; unsigned d_ = cvtpk(a_, b_); PIN(d_); PIN(LL); DST = d_; } while (0)
      const lds_cptr ka = (lds_cptr)(uintptr_t)kA_, kb = (lds_cptr)(uintptr_t)kB_;
      bf16x8 kf[16];
#define KRD(i) kf[i] = ldl((((i) >> 3) ? kb : ka) + 4096 * (((i) >> 2) & 1) + 512 * ((i) & 3))
      KRD(0); KRD(1); KRD(2);
      SB();
#pragma unroll
      for (int i = 0; i < 8; ++i) {
        const int kt = i >> 2, ks = i & 3;
        KRD(i + 3);
        if (ks == 0) {
          s[0][kt] = mfma16_head(kf[i], qf[0][0], negm[0]);
          s[1][kt] = mfma16_head(kf[i], qf[1][0], negm[1]);
          se[kt] = mfma16_head(kf[i], qe[0], negme);
        } else {
          s[0][kt] = MFMA16(kf[i], qf[0][ks], s[0][kt]);
          s[1][kt] = MFMA16(kf[i], qf[1][ks], s[1][kt]);
          se[kt] = MFMA16(kf[i], qe[ks], se[kt]);
        }
        if (i == 1) PIECE_V(0);
        if (i == 5) PIECE_V(1);
        if (i == 5) { m0 = MX3(s[0][0][0], s[0][0][1], s[0][0][2]); m1 = MX3(s[1][0][0], s[1][0][1], s[1][0][2]); PIN(m0); PIN(m1); }
        if (i == 6) { me = MX3(se[0][0], se[0][1], se[0][2]); m0 = MX3(m0, s[0][0][3], m0); PIN(me); PIN(m0); }
        if (i == 7) { m1 = MX3(m1, s[1][0][3], m1); me = MX3(me, se[0][3], me); PIN(m1); PIN(me); }
        SB();
      }
      m0 = MX3(m0, s[0][1][0], s[0][1][1]); m0 = MX3(m0, s[0][1][2], s[0][1][3]);
      m1 = MX3(m1, s[1][1][0], s[1][1][1]); m1 = MX3(m1, s[1][1][2], s[1][1][3]);
      me = MX3(me, se[1][0], se[1][1]); me = MX3(me, se[1][2], se[1][3]);
      if (__builtin_expect(__any(MX3(m0, m1, me) > thr), 0)) {
        const float d0 = fmaxf(rowmax4(m0), lo), d1 = fmaxf(rowmax4(m1), lo), de = fmaxf(rowmax4(me), lo);
        const float a0 = EX(-fmaxf(d0, 0.f)), a1 = EX(-fmaxf(d1, 0.f)), ae_ = EX(-fmaxf(de, 0.f));
        l0 *= a0; l1 *= a1; le *= ae_;
#pragma unroll
        for (int d = 0; d < 8; ++d) { o[0][d] *= a0; o[1][d] *= a1; oe[d] *= ae_; }
        s[0][0] -= d0; s[0][1] -= d0; s[1][0] -= d1; s[1][1] -= d1; se[0] -= de; se[1] -= de;
        negm[0] -= d0; negm[1] -= d1; negme -= de;
        PIN(negm[0]); PIN(negm[1]); PIN(negme);
        asm volatile("s_nop 1");
      }
      SB();
#pragma unroll
      for (int i = 0; i < 8; ++i) {
        const int kt = 2 + (i >> 2), ks = i & 3;
        if (i + 11 < 16) { KRD(i + 11); }
        if (ks == 0) {
          s[0][kt] = mfma16_head(kf[8 + i], qf[0][0], negm[0]);
          s[1][kt] = mfma16_head(kf[8 + i], qf[1][0], negm[1]);
        } else {
          s[0][kt] = MFMA16(kf[8 + i], qf[0][ks], s[0][kt]);
          s[1][kt] = MFMA16(kf[8 + i], qf[1][ks], s[1][kt]);
        }
        if (i < 4) P2(s[0][i >> 1], 2 * (i & 1), l0, pw[0][0][i]);
        else P2(s[1][(i - 4) >> 1], 2 * (i & 1), l1, pw[1][0][i - 4]);
        if (i & 1) P2(se[i >> 2], 2 * ((i >> 1) & 1), le, pwe[i >> 1]);
        if (i == 2) PIECE_V(2);
        if (i == 6) PIECE_V(3);
        SB();
      }
#undef KRD
      m0 = MX3(s[0][2][0], s[0][2][1], s[0][2][2]); m0 = MX3(m0, s[0][2][3], s[0][3][0]); m0 = MX3(m0, s[0][3][1], s[0][3][2]);
      m1 = MX3(s[1][2][0], s[1][2][1], s[1][2][2]); m1 = MX3(m1, s[1][2][3], s[1][3][0]); m1 = MX3(m1, s[1][3][1], s[1][3][2]);
      m0 = MX3(m0, s[0][3][3], m0); m1 = MX3(m1, s[1][3][3], m1);
      if (__builtin_expect(__any(MX3(m0, m1, m1) > THR_L2), 0)) {
        const float d0 = fmaxf(rowmax4(m0), 0.f), d1 = fmaxf(rowmax4(m1), 0.f);
        const float a0 = EX(-d0), a1 = EX(-d1);
        l0 *= a0; l1 *= a1;
#pragma unroll
        for (int d = 0; d < 8; ++d) { o[0][d] *= a0; o[1][d] *= a1; }
#pragma unroll
        for (int i = 0; i < 4; ++i) {
          const unsigned v0 = pw[0][0][i], v1 = pw[1][0][i];
          pw[0][0][i] = cvtpk(__uint_as_float(v0 << 16) * a0, __uint_as_float(v0 & 0xffff0000u) * a0);
          pw[1][0][i] = cvtpk(__uint_as_float(v1 << 16) * a1, __uint_as_float(v1 & 0xffff0000u) * a1);
        }
        s[0][2] -= d0; s[0][3] -= d0; s[1][2] -= d1; s[1][3] -= d1;
        negm[0] -= d0; negm[1] -= d1;
      }
      STAMP(2);
      WAIT_BAR(8);
      STAMP(3);
      if constexpr (!FAST) prep(nk + 2 * t + 4);
      {
        const lds_cptr a0 = (lds_cptr)(uintptr_t)vA0, a1 = (lds_cptr)(uintptr_t)vA1, b0 = (lds_cptr)(uintptr_t)vB0, b1 = (lds_cptr)(uintptr_t)vB1;
        bf16x8 vf[16];
#define VB(i) (((i) >> 3) ? (((i) & 1) ? b1 : b0) : (((i) & 1) ? a1 : a0))
#define VRD(i) vf[i] = tr2(VB(i) + 512 * (((i) & 7) >> 1), VB(i) + 512 * (((i) & 7) >> 1) + 4096)
        VRD(0); VRD(1);
        SB();
#pragma unroll
        for (int i = 0; i < 8; ++i) {
          VRD(i + 2);
          o[0][i] = MFMA16(vf[i], __builtin_bit_cast(bf16x8, pw[0][0]), o[0][i]);
          o[1][i] = MFMA16(vf[i], __builtin_bit_cast(bf16x8, pw[1][0]), o[1][i]);
          oe[i] = MFMA16(vf[i], __builtin_bit_cast(bf16x8, pwe), oe[i]);
          if (i < 4) P2(s[0][2 + (i >> 1)], 2 * (i & 1), l0, pw[0][1][i]);
          else P2(s[1][2 + ((i - 4) >> 1)], 2 * (i & 1), l1, pw[1][1][i - 4]);
          if (i == 1) PIECE_K(0);
          if (i == 5) PIECE_K(1);
          SB();
        }
#pragma unroll
        for (int i = 0; i < 8; ++i) {
          if (i + 10 < 16) { VRD(i + 10); }
          o[0][i] = MFMA16(vf[8 + i], __builtin_bit_cast(bf16x8, pw[0][1]), o[0][i]);
          o[1][i] = MFMA16(vf[8 + i], __builtin_bit_cast(bf16x8, pw[1][1]), o[1][i]);
          if (i == 1) PIECE_K(2);
          if (i == 5) PIECE_K(3);
          SB();
        }
#undef VB
#undef VRD
      }
#undef EX
#undef P2
      STAMP(5);
      const unsigned dir = (t & 1) ? 0u - 2 * SLOT : 2 * SLOT;
      kA_ += dir; kB_ += dir; vA0 += dir; vA1 += dir; vB0 += dir; vB1 += dir;
      PIN(kA_); PIN(kB_); PIN(vA0); PIN(vA1); PIN(vB0); PIN(vB1);
#undef PIECE_V
#undef PIECE_K
    };
#pragma unroll 1
    for (int t = 0; t < 14; ++t) tile(t, std::true_type{});
#pragma unroll 1
    for (int t = 14; t < 16; ++t) tile(t, std::false_type{});

    char* ob = (char*)o_ws + (size_t)((u * NB + b) * NH + h) * (40 * 1024);
    const int ln = lane_id();
    int w = w_;
    asm volatile("" : "+s"(w));
    const int eg = w & 1, kh = w >> 1;
    {
      const float i0 = 1.0f / rowsum4(l0), i1 = 1.0f / rowsum4(l1);
      char* of = ob + w * 8192 + 16 * ln;
#pragma unroll
      for (int uu = 0; uu < 4; ++uu) {
        *(bf16x8*)(of + 1024 * uu) = pack44(o[0][2 * uu] * i0, o[0][2 * uu + 1] * i0);
        *(bf16x8*)(of + 4096 + 1024 * uu) = pack44(o[1][2 * uu] * i1, o[1][2 * uu + 1] * i1);
      }
    }
    {
      constexpr unsigned EX0 = 3 * SLOT;
      const int r16 = ln & 15;
      const float me = -negme[0];
      le = rowsum4(le);
      LDS_BAR();
      if (ln < 16) {
        f32x2 ml = {me, le};
        *(__attribute__((address_space(3))) f32x2*)(LW + EX0 + 4096 * (w ^ 2) + 8 * ln) = ml;
      }
      LDS_BAR();
      f32x2 pml = *(const __attribute__((address_space(3))) f32x2*)(L + EX0 + 4096 * w + 8 * r16);
      const float M = fmaxf(me, pml[0]);
      const float sc = __builtin_amdgcn_exp2f(me - M), scp = __builtin_amdgcn_exp2f(pml[0] - M);
      const float f = sc / (le * sc + pml[1] * scp);
      asm volatile("s_waitcnt lgkmcnt(0)" ::: "memory");
      f32x4 keep[4];
#pragma unroll
      for (int i = 0; i < 4; ++i) {
        f32x4 snd;
#pragma unroll
        for (int r = 0; r < 4; ++r) {
          snd[r] = (kh ? oe[i][r] : oe[4 + i][r]) * f;
          keep[i][r] = (kh ? oe[4 + i][r] : oe[i][r]) * f;
        }
        *(__attribute__((address_space(3))) f32x4*)(LW + EX0 + 4096 * w + 1024 * i + 16 * ln) = snd;
      }
      LDS_BAR();
#pragma unroll
      for (int i = 0; i < 4; ++i) keep[i] += *(const __attribute__((address_space(3))) f32x4*)(L + EX0 + 4096 * (w ^ 2) + 1024 * i + 16 * ln);
      char* ef = ob + 32768 + 4096 * eg + 2048 * kh + 16 * ln;
      *(bf16x8*)(ef) = pack44(keep[0], keep[1]);
      *(bf16x8*)(ef + 1024) = pack44(keep[2], keep[3]);
    }
  };

#pragma unroll 1
  for (int i = 0; i < (tb ? 2 : 1); ++i) qphase((tb && i == 0) ? 1 : 0, 16 * i, tb && i == 0);
  attention(std::integral_constant<int, 0>{}, tb ? 32 : 16);
  __builtin_amdgcn_s_setprio(0);
  if (tb) {
    const int ln = lane_id();
#pragma unroll
    for (int a = 0; a < 2; ++a)
#pragma unroll
      for (int s = 0; s < 4; ++s) qf[a][s] = *(const bf16x8*)(qpark + w * 8192 + 1024 * (4 * a + s) + 16 * ln);
#pragma unroll
    for (int s = 0; s < 4; ++s) qe[s] = *(const bf16x8*)(qpark + 32768 + 256 * (16 * eg + (ln & 15)) + 16 * (4 * s + (ln >> 4)));
#pragma unroll
    for (int a = 0; a < 2; ++a)
#pragma unroll
      for (int s = 0; s < 4; ++s) PIN(qf[a][s]);
#pragma unroll
    for (int s = 0; s < 4; ++s) PIN(qe[s]);
  } else {
    qphase(1, 48, false);
  }
  {
    const char* wp = (const char*)(g & 1 ? woB : woA) + (size_t)(g >> 1) * 16384 + tid * 16;
    u32x4 t0 = *(const u32x4*)(wp), t1 = *(const u32x4*)(wp + 4096), t2 = *(const u32x4*)(wp + 8192), t3 = *(const u32x4*)(wp + 12288);
    asm volatile("" ::"v"(t0), "v"(t1), "v"(t2), "v"(t3));
  }
  attention(std::integral_constant<int, 1>{}, 64);
  asm volatile("s_waitcnt vmcnt(0)" ::: "memory");
}

constexpr unsigned S2 = 36864, NS2 = 4, LDS2 = S2 * NS2;

__global__ __launch_bounds__(768, 3) void oproj_kernel(char* __restrict__ ws, const u16* __restrict__ woA, const u16* __restrict__ woB,
                                                       u16* __restrict__ out) {
  extern __shared__ __attribute__((aligned(16))) char smem[];
  const int tid = threadIdx.x, lane = tid & 63;
  const int w = __builtin_amdgcn_readfirstlane(tid >> 6);
  const int wn = w & 3, wk = w >> 2;
  const int g = blockIdx.x, xcd = g & 7, idx = g >> 3;
  const int blkk = xcd >> 2, mt = 4 * (xcd & 3) + (idx >> 3), nt = (idx >> 1) & 3, kh = idx & 1;
  const int tile = (blkk * 16 + mt) * 4 + nt;
  const unsigned lds0 = (unsigned)(uintptr_t)smem;
  const lds_cptr L = (lds_cptr)smem;
  const lds_ptr LW = (lds_ptr)smem;
  const int r16 = lane & 15, gq = lane >> 4, q4 = (lane & 15) >> 2, p4 = lane & 3;
  if (w >= 8) {
    const char* so_ = (const char*)ws + (size_t)((blkk * NB + mt) * NH + kh * 16) * (40 * 1024) + 4096 * (wn >> 1) + 1024 * (wn & 1);
    const char* sw_ = (const char*)((blkk ? woB : woA) + (size_t)(kh * 2048) * DM + nt * 128);
    const __amdgpu_buffer_rsrc_t rso = __builtin_amdgcn_make_buffer_rsrc((void*)so_, (short)0, 4 << 20, 0x00020000);
    const __amdgpu_buffer_rsrc_t rsw = __builtin_amdgcn_make_buffer_rsrc((void*)sw_, (short)0, 4 << 20, 0x00020000);
    const unsigned vo = 16 * lane;
    const unsigned vw = (unsigned)(8 * (wn >> 1) + ((lane >> 2) & 7)) * (DM * 2) + 128 * (wn & 1) + 64 * (lane >> 5) + 16 * ((lane & 3) ^ (2 * ((lane >> 4) & 1)));
    auto chunk = [&](int c) {
      const unsigned cc = (unsigned)c & 31u;
      const unsigned oo = (cc >> 1) * 40960u + (cc & 1) * 2048u, ow = cc * (64u * DM * 2);
      const unsigned dst = lds0 + (unsigned)wn * 1024 + (unsigned)(c & 3) * S2;
      bdma<0, 0, false>(rso, vo, oo, dst);
      bdma<20480, 0, false>(rsw, vw, ow, dst);
      bdma<4096, 8192, false>(rso, vo, oo, dst);
      bdma<20480 + 4096, 16 * DM * 2, false>(rsw, vw, ow, dst);
      bdma<8192, 16384, false>(rso, vo, oo, dst);
      bdma<20480 + 8192, 32 * DM * 2, false>(rsw, vw, ow, dst);
      bdma<12288, 24576, false>(rso, vo, oo, dst);
      bdma<20480 + 12288, 48 * DM * 2, false>(rsw, vw, ow, dst);
      bdma<16384, 32768, false>(rso, vo, oo, dst);
    };
    chunk(0);
    chunk(1);
    chunk(2);
#pragma unroll 1
    for (int c = 0; c < 32; ++c) {
      WAIT_BAR(18);
      chunk(c + 3);
    }
    WAIT_BAR(0);
    return;
  }
  const unsigned ob0 = 1024 * wk + 16 * lane;
  const unsigned wb0 = 20480 + 2048 * (4 * wk + (gq >> 1)) + 512 * wn + 64 * (4 * (gq & 1) + q4) + 32 * (0 ^ (gq & 1)) + 8 * p4;
  const unsigned wb1 = 20480 + 2048 * (4 * wk + (gq >> 1)) + 512 * wn + 64 * (4 * (gq & 1) + q4) + 32 * (1 ^ (gq & 1)) + 8 * p4;
#define DUMP()
  f32x4 acc[2][10] = {};
  STAMP(0);
#pragma unroll 1
  for (int c = 0; c < 32; ++c) {
    LDS_BAR();
    const lds_cptr sl = L + (c & 3) * S2;
    bf16x8 fa0 = tr2(sl + wb0, sl + wb0 + 4096), fa1 = tr2(sl + wb1, sl + wb1 + 4096);
    bf16x8 fb[10];
    fb[0] = ldl(sl + ob0);
    fb[1] = ldl(sl + ob0 + 2048);
    fb[2] = ldl(sl + ob0 + 4096);
    SB();
#pragma unroll
    for (int k = 0; k < 10; ++k) {
      if (k + 3 < 10) fb[k + 3] = ldl(sl + ob0 + 2048 * (k + 3));
      acc[0][k] = MFMA16(fa0, fb[k], acc[0][k]);
      acc[1][k] = MFMA16(fa1, fb[k], acc[1][k]);
      SB();
    }
  }
  STAMP(1);
  WAIT_BAR(0);
  f32x4 res[10];
  auto red = [&](auto K_) __attribute__((always_inline)) {
    constexpr int kk = decltype(K_)::value;
#pragma unroll
    for (int i = 0; i < 10; ++i) *(__attribute__((address_space(3))) f32x4*)(LW + 10240 * w + 1024 * i + 16 * lane) = acc[1 - kk][i];
    LDS_BAR();
#pragma unroll
    for (int i = 0; i < 10; ++i) res[i] = acc[kk][i] + *(const __attribute__((address_space(3))) f32x4*)(L + 10240 * (w ^ 4) + 1024 * i + 16 * lane);
  };
  if (wk) red(std::integral_constant<int, 1>{});
  else red(std::integral_constant<int, 0>{});
  STAMP(2);
  unsigned* sync = (unsigned*)(ws + WS_SYNC + (size_t)tile * 128);
  volatile __attribute__((address_space(3))) unsigned* tk = (volatile __attribute__((address_space(3))) unsigned*)(LW + 90112);
  const __amdgpu_buffer_rsrc_t rs = __builtin_amdgcn_make_buffer_rsrc(ws + WS_PART + (size_t)tile * 163840, 0, 163840, 0x00020000);
  const unsigned poff = (unsigned)w * 10240 + 16 * lane;
  const bool mine = (wn >> 1) == kh;
  auto publish = [&]() {
#pragma unroll
    for (int i = 0; i < 10; ++i) __builtin_amdgcn_raw_buffer_store_b128(__builtin_bit_cast(u32x4, res[i]), rs, 81920u * kh + poff + 1024 * i, 0, 16);
  };
  if (!mine) publish();
  asm volatile("s_waitcnt vmcnt(0)" ::: "memory");
  __builtin_amdgcn_s_barrier();
  if (tid == 0) {
    __hip_atomic_store(sync + kh, 1u, __ATOMIC_RELAXED, __HIP_MEMORY_SCOPE_AGENT);
    unsigned st = 0u, spins = 0;
    while (__hip_atomic_load(sync + (kh ^ 1), __ATOMIC_RELAXED, __HIP_MEMORY_SCOPE_AGENT) == 0u) {
      __builtin_amdgcn_s_sleep(1);
      if (++spins == SPIN_MAX) { st = 1u; break; }
    }
    *tk = st;
  }
  LDS_BAR();
  unsigned st = __builtin_amdgcn_readfirstlane(*tk);
  if (st == 1u) {
    if (mine) publish();
    asm volatile("s_waitcnt vmcnt(0)" ::: "memory");
    __builtin_amdgcn_s_barrier();
    if (tid == 0) {
      unsigned e1 = 0u;
      __hip_atomic_compare_exchange_strong(sync + 16 + (kh ^ 1), &e1, 2u, __ATOMIC_RELAXED, __ATOMIC_RELAXED, __HIP_MEMORY_SCOPE_AGENT);
      bool gave = false;
      if (e1 != 1u) {
        unsigned e2 = 0u;
        gave = __hip_atomic_compare_exchange_strong(sync + 16 + kh, &e2, 1u, __ATOMIC_RELAXED, __ATOMIC_RELAXED, __HIP_MEMORY_SCOPE_AGENT);
      }
      if (!gave)
        while (__hip_atomic_load(sync + (kh ^ 1), __ATOMIC_RELAXED, __HIP_MEMORY_SCOPE_AGENT) == 0u) __builtin_amdgcn_s_sleep(1);
      *tk = gave ? 1u : 0u;
    }
    LDS_BAR();
    st = __builtin_amdgcn_readfirstlane(*tk);
    if (st == 1u) return;
  }
  __builtin_amdgcn_fence(__ATOMIC_ACQUIRE, "wavefront");
  STAMP(4);
  auto finish = [&]() {
#pragma unroll
    for (int i = 0; i < 10; ++i) {
      u16* orow = out + (size_t)(mt * 160 + 16 * i + r16) * 1024 + blkk * 512 + nt * 128 + 32 * wn + 16 * wk + 4 * gq;
      const f32x4 v = __builtin_bit_cast(f32x4, __builtin_amdgcn_raw_buffer_load_b128(rs, 81920u * (kh ^ 1) + poff + 1024 * i, 0, 16));
      u32x2 pv = {cvtpk(res[i][0] + v[0], res[i][1] + v[1]), cvtpk(res[i][2] + v[2], res[i][3] + v[3])};
      *(u32x2*)orow = pv;
    }
  };
  if (mine) finish();
  if (tid == 64 * (2 * (kh ^ 1))) {
    unsigned expect = 0u;
    __hip_atomic_compare_exchange_strong(sync + 16 + (kh ^ 1), &expect, 2u, __ATOMIC_RELAXED, __ATOMIC_RELAXED, __HIP_MEMORY_SCOPE_AGENT);
    tk[1] = expect;
  }
  LDS_BAR();
  if (!mine && __builtin_amdgcn_readfirstlane(tk[1]) == 1u) {
    __builtin_amdgcn_fence(__ATOMIC_ACQUIRE, "wavefront");
    finish();
  }
  STAMP(5);
  DUMP();
}

extern "C" void kernel_launch(void* const* d_in, const int* in_sizes, int n_in, void* d_out, int out_size, void* d_ws, size_t ws_size,
                              hipStream_t stream) {
  const u16* xA = (const u16*)d_in[0];
  const u16* kA = (const u16*)d_in[1];
  const u16* vA = (const u16*)d_in[2];
  const u16* wqA = (const u16*)d_in[3];
  const u16* woA = (const u16*)d_in[4];
  const u16* xB = (const u16*)d_in[5];
  const u16* kB = (const u16*)d_in[6];
  const u16* vB = (const u16*)d_in[7];
  const u16* wqB = (const u16*)d_in[8];
  const u16* woB = (const u16*)d_in[9];
  u16* o_ws = (u16*)d_ws;
  (void)hipFuncSetAttribute((const void*)attn_kernel, hipFuncAttributeMaxDynamicSharedMemorySize, (int)LDS1);
  (void)hipFuncSetAttribute((const void*)oproj_kernel, hipFuncAttributeMaxDynamicSharedMemorySize, (int)LDS2);
  hipLaunchKernelGGL(attn_kernel, dim3(512), dim3(256), LDS1, stream, xA, kA, vA, wqA, xB, kB, vB, wqB, o_ws, woA, woB);
  hipLaunchKernelGGL(oproj_kernel, dim3(256), dim3(768), LDS2, stream, (char*)d_ws, woA, woB, (u16*)d_out);
}
```
